```python
import torch

def _softmax(x, axis=-1):
    return torch.softmax(x, dim=axis)
def _gelu(x):
    return torch.nn.functional.gelu(x, approximate="tanh")
def _silu(x):
    return torch.nn.functional.silu(x)
def _sigmoid(x):
    return torch.sigmoid(x)
def _layernorm(x, axis=-1, eps=1e-5):
    m = x.mean(dim=axis, keepdim=True)
    v = x.var(dim=axis, keepdim=True, unbiased=False)
    return (x - m) / torch.sqrt(v + eps)
def _scatter_add(tgt, idx, src, axis=0):
    return tgt.clone().index_add_(axis, idx, src)
def _segment_sum(src, seg, n):
    out = torch.zeros((n,) + src.shape[1:], dtype=src.dtype)
    return out.index_add_(0, seg, src)
def _pad(x, pw):
    flat = tuple(v for pair in reversed(pw) for v in pair)
    return torch.nn.functional.pad(x, flat)

kernel_name = 'concat_heads__gqa_block_cached__gqa_block_cached__near_ridge'

def setup_inputs():

    _w = (lambda a: (lambda t: t.to(torch.bfloat16) if t.is_floating_point() else t)(torch.as_tensor(a)))
    def _A_setup():
        B, Sq, D = 8, 32, 512
        Hq, Hkv, Sk, Dh = 32, 8, 2048, 128
        assert Hq % Hkv == 0, (Hq, Hkv)
        return {
            "x": _w((lambda *s: torch.randn(*s, dtype=torch.bfloat16) * 0.8)(B, Sq, D)),
            "k_cache": _w((lambda *s: torch.randn(*s, dtype=torch.bfloat16) * 0.8)(B, Hkv, Sk, Dh)),
            "v_cache": _w((lambda *s: torch.randn(*s, dtype=torch.bfloat16) * 0.05)(B, Hkv, Sk, Dh)),
            "wq": _w((lambda *s: torch.randn(*s, dtype=torch.bfloat16) * 0.8)(D, Hq * Dh) * (D ** -0.5)),
            "wo": _w((lambda *s: torch.randn(*s, dtype=torch.bfloat16) * 0.05)(Hq * Dh, D) * ((Hq * Dh) ** -0.5)),
        }
    _A_in = _A_setup()

    def _B_setup():
        B, Sq, D = 8, 32, 512
        Hq, Hkv, Sk, Dh = 32, 8, 2048, 128
        assert Hq % Hkv == 0, (Hq, Hkv)
        return {
            "x": _w((lambda *s: torch.randn(*s, dtype=torch.bfloat16) * 0.8)(B, Sq, D)),
            "k_cache": _w((lambda *s: torch.randn(*s, dtype=torch.bfloat16) * 0.8)(B, Hkv, Sk, Dh)),
            "v_cache": _w((lambda *s: torch.randn(*s, dtype=torch.bfloat16) * 0.05)(B, Hkv, Sk, Dh)),
            "wq": _w((lambda *s: torch.randn(*s, dtype=torch.bfloat16) * 0.8)(D, Hq * Dh) * (D ** -0.5)),
            "wo": _w((lambda *s: torch.randn(*s, dtype=torch.bfloat16) * 0.05)(Hq * Dh, D) * ((Hq * Dh) ** -0.5)),
        }
    _B_in = _B_setup()

    return {
        "A_x": _A_in["x"],
        "A_k_cache": _A_in["k_cache"],
        "A_v_cache": _A_in["v_cache"],
        "A_wq": _A_in["wq"],
        "A_wo": _A_in["wo"],
        "B_x": _B_in["x"],
        "B_k_cache": _B_in["k_cache"],
        "B_v_cache": _B_in["v_cache"],
        "B_wq": _B_in["wq"],
        "B_wo": _B_in["wo"],
    }

def reference(A_x, A_k_cache, A_v_cache, A_wq, A_wo, B_x, B_k_cache, B_v_cache, B_wq, B_wo):
    def _A_ref(x, k_cache, v_cache, wq, wo):
        B, Sq, D = x.shape
        Hq, Dh = 32, 128
        Hkv = k_cache.shape[1]
        rep = Hq // Hkv
        q = (lambda x, *d: x.permute(*d))((x @ wq).reshape(B, Sq, Hq, Dh), 0, 2, 1, 3)
        k = (lambda x, n, axis: torch.repeat_interleave(x, n, dim=axis))(k_cache, rep, axis=1)
        v = (lambda x, n, axis: torch.repeat_interleave(x, n, dim=axis))(v_cache, rep, axis=1)
        scores = torch.einsum("bhqd,bhkd->bhqk", q, k) * (Dh ** -0.5)
        p = _softmax(scores, axis=-1)
        o = torch.einsum("bhqk,bhkd->bhqd", p, v)
        return (lambda x, *d: x.permute(*d))(o, 0, 2, 1, 3).reshape(B, Sq, Hq * Dh) @ wo

    def _B_ref(x, k_cache, v_cache, wq, wo):
        B, Sq, D = x.shape
        Hq, Dh = 32, 128
        Hkv = k_cache.shape[1]
        rep = Hq // Hkv
        q = (lambda x, *d: x.permute(*d))((x @ wq).reshape(B, Sq, Hq, Dh), 0, 2, 1, 3)
        k = (lambda x, n, axis: torch.repeat_interleave(x, n, dim=axis))(k_cache, rep, axis=1)
        v = (lambda x, n, axis: torch.repeat_interleave(x, n, dim=axis))(v_cache, rep, axis=1)
        scores = torch.einsum("bhqd,bhkd->bhqk", q, k) * (Dh ** -0.5)
        p = _softmax(scores, axis=-1)
        o = torch.einsum("bhqk,bhkd->bhqd", p, v)
        return (lambda x, *d: x.permute(*d))(o, 0, 2, 1, 3).reshape(B, Sq, Hq * Dh) @ wo

    a = _A_ref(A_x, A_k_cache, A_v_cache, A_wq, A_wo)
    b = _B_ref(B_x, B_k_cache, B_v_cache, B_wq, B_wo)
    return torch.concatenate([a, b], axis=-1)
```

```cpp
#include <hip/hip_runtime.h>
#include <hip/hip_bf16.h>
typedef unsigned short u16;
__device__ __forceinline__ float bf2f(u16 b){ return __uint_as_float(((unsigned)b)<<16); }
__device__ __forceinline__ u16 f2bf(float f){ unsigned u=__float_as_uint(f); u += 0x7fffu + ((u>>16)&1u); return (u16)(u>>16); }
constexpr int B=8,SQ=32,D=512,HQ=32,HKV=8,SK=2048,DH=128;
__global__ void qproj(const u16* x, const u16* wq, u16* q){
  int n = blockIdx.x*256+threadIdx.x;
  int row = blockIdx.y;
  float acc=0;
  for(int k=0;k<D;k++) acc += bf2f(x[row*D+k])*bf2f(wq[(size_t)k*4096+n]);
  q[(size_t)row*4096+n]=f2bf(acc);
}
__global__ void attn(const u16* q, const u16* k, const u16* v, u16* o){
  int bh = blockIdx.x; int b = bh/HQ, h = bh%HQ, kvh = h/4; int qi = blockIdx.y;
  __shared__ float qs[DH]; __shared__ float s[SK]; __shared__ float red[256];
  int t=threadIdx.x;
  if(t<DH) qs[t]=bf2f(q[(size_t)(b*SQ+qi)*4096+h*DH+t]);
  __syncthreads();
  const u16* kb = k + (size_t)(b*HKV+kvh)*SK*DH; const u16* vb = v + (size_t)(b*HKV+kvh)*SK*DH;
  float mx=-1e30f;
  for(int j=t;j<SK;j+=256){ float a=0; for(int d=0;d<DH;d++) a+=qs[d]*bf2f(kb[(size_t)j*DH+d]); a*=0.08838834764831845f; s[j]=a; mx=fmaxf(mx,a);}
  red[t]=mx; __syncthreads();
  for(int w=128;w>0;w>>=1){ if(t<w) red[t]=fmaxf(red[t],red[t+w]); __syncthreads(); }
  mx=red[0]; __syncthreads();
  float sum=0; for(int j=t;j<SK;j+=256){ float e=__expf(s[j]-mx); s[j]=e; sum+=e; }
  red[t]=sum; __syncthreads();
  for(int w=128;w>0;w>>=1){ if(t<w) red[t]+=red[t+w]; __syncthreads(); }
  sum=red[0];
  if(t<DH){ float a=0; for(int j=0;j<SK;j++) a+=s[j]*bf2f(vb[(size_t)j*DH+t]); o[(size_t)(b*SQ+qi)*4096+h*DH+t]=f2bf(a/sum); }
}
__global__ void oproj(const u16* o, const u16* wo, u16* out, int p){
  int n = blockIdx.x*256+threadIdx.x;
  int row = blockIdx.y;
  float acc=0;
  for(int k=0;k<4096;k++) acc += bf2f(o[(size_t)row*4096+k])*bf2f(wo[(size_t)k*512+n]);
  out[(size_t)row*1024+p*512+n]=f2bf(acc);
}
extern "C" void kernel_launch(void* const* d_in, const int* in_sizes, int n_in, void* d_out, int out_size, void* d_ws, size_t ws_size, hipStream_t stream){
  u16* ws=(u16*)d_ws;
  for(int p=0;p<2;p++){
    const u16* x=(const u16*)d_in[p*5+0]; const u16* k=(const u16*)d_in[p*5+1]; const u16* v=(const u16*)d_in[p*5+2]; const u16* wq=(const u16*)d_in[p*5+3]; const u16* wo=(const u16*)d_in[p*5+4];
    u16* q=ws+(size_t)p*2*256*4096; u16* o=q+256*4096;
    qproj<<<dim3(16,256),256,0,stream>>>(x,wq,q);
    attn<<<dim3(256,32),256,0,stream>>>(q,k,v,o);
    oproj<<<dim3(2,256),256,0,stream>>>(o,wo,(u16*)d_out,p);
  }
}
```

```cpp
#include <hip/hip_runtime.h>
#include <stdint.h>

typedef unsigned u32;
typedef unsigned long long u64;
typedef u32 u32x4 __attribute__((ext_vector_type(4)));
typedef u32 u32x2 __attribute__((ext_vector_type(2)));
typedef float f32x2 __attribute__((ext_vector_type(2)));
typedef float f32x4 __attribute__((ext_vector_type(4)));
typedef float f32x16 __attribute__((ext_vector_type(16)));
typedef __bf16 bf16x2 __attribute__((ext_vector_type(2)));
typedef __bf16 bf16x4 __attribute__((ext_vector_type(4)));
typedef __bf16 bf16x8 __attribute__((ext_vector_type(8)));
#define LAS __attribute__((address_space(3)))
#define GAS __attribute__((address_space(1)))
#define DI __device__ __forceinline__
#define RLX_AGENT __ATOMIC_RELAXED, __HIP_MEMORY_SCOPE_AGENT
#define SBAR() __builtin_amdgcn_sched_barrier(0)
#define PIN(x) asm volatile("" : "+v"(x))

constexpr u32 RING = 0, BUFB = 32768, SCR = 131072, MISC = SCR + 16384, LDS_BYTES = 163840;
constexpr u32 WS_EP = 0;
constexpr u32 WS_FQ = 4096;
constexpr u32 WS_FO = WS_FQ + 32768;
constexpr u32 WS_F3 = WS_FO + 32768;
constexpr u32 WS_QX = 131072;
constexpr u32 OXH = 16384 + 256;
constexpr u32 WS_OX = WS_QX + 256u * 16384u;
constexpr u32 WS_SL = WS_OX + 256u * 2u * OXH;
constexpr u32 WS_END = WS_SL + 16u * 16u * 16u * 2048u;
constexpr u32 WS_DBG = WS_END;
constexpr u64 TAGC = 0x9E3779B97F4A7C15ull;
constexpr float CSC = 0.088388347648318447f * 1.4426950408889634f;
constexpr float THR = 8.0f;
constexpr int AUX_SC1 = 16;
constexpr u32 SPIN_CAP = 1u << 18;

struct Params {
  const void* x[2]; const void* k[2]; const void* v[2]; const void* wq[2]; const void* wo[2];
  void* out; void* ws;
};

#define BAR() asm volatile("s_waitcnt lgkmcnt(0)\n\ts_barrier" ::: "memory")
#define DRAIN() asm volatile("s_waitcnt vmcnt(0)" ::: "memory")
template <int N> DI void waitvm() { asm volatile("s_waitcnt vmcnt(%0)" ::"i"(N) : "memory"); }

DI void dma(u32 m0v, u32 voff, __amdgpu_buffer_rsrc_t srd, u32 soff) {
  asm volatile("s_mov_b32 m0, %0\n\ts_nop 0\n\tbuffer_load_dwordx4 %1, %2, %3 offen nt lds" ::"s"(m0v), "v"(voff), "s"(srd), "s"(soff) : "memory");
}
template <int OFF> DI void dma_o(u32 m0v, u32 voff, __amdgpu_buffer_rsrc_t srd, u32 soff) {
  asm volatile("s_mov_b32 m0, %0\n\ts_nop 0\n\tbuffer_load_dwordx4 %1, %2, %3 offen offset:%4 nt lds" ::"s"(m0v), "v"(voff), "s"(srd), "s"(soff), "i"(OFF) : "memory");
}
DI u32 cvtpk(float lo, float hi) { f32x2 v = {lo, hi}; bf16x2 b = __builtin_convertvector(v, bf16x2); return __builtin_bit_cast(u32, b); }
DI bf16x8 pack8(const f32x16& x, int s) {
  u32x4 w = {cvtpk(x[8 * s], x[8 * s + 1]), cvtpk(x[8 * s + 2], x[8 * s + 3]), cvtpk(x[8 * s + 4], x[8 * s + 5]), cvtpk(x[8 * s + 6], x[8 * s + 7])};
  return __builtin_bit_cast(bf16x8, w);
}
DI bf16x8 lds128(u32 a) { return *(const LAS bf16x8*)(size_t)a; }
DI void lds_st128(u32 a, bf16x8 v) { *(LAS bf16x8*)(size_t)a = v; }
DI bf16x8 trfrag(u32 a0, u32 a1) {
  bf16x4 lo = __builtin_amdgcn_ds_read_tr16_b64_v4bf16((LAS bf16x4*)(size_t)a0);
  bf16x4 hi = __builtin_amdgcn_ds_read_tr16_b64_v4bf16((LAS bf16x4*)(size_t)a1);
  return __builtin_shufflevector(lo, hi, 0, 1, 2, 3, 4, 5, 6, 7);
}
#define MFMA(a, b, c) __builtin_amdgcn_mfma_f32_32x32x16_bf16((a), (b), (c), 0, 0, 0)
DI float swap_max(float v) { auto r = __builtin_amdgcn_permlane32_swap(__float_as_uint(v), __float_as_uint(v), false, false); return fmaxf(__uint_as_float(r[0]), __uint_as_float(r[1])); }
DI float swap_add(float v) { auto r = __builtin_amdgcn_permlane32_swap(__float_as_uint(v), __float_as_uint(v), false, false); return __uint_as_float(r[0]) + __uint_as_float(r[1]); }
DI float max3(float a, float b, float c) { float r; asm("v_max3_f32 %0, %1, %2, %3" : "=v"(r) : "v"(a), "v"(b), "v"(c)); return r; }

DI void poll_flags(const GAS u64* base, int mask, int lane, u64 tag) {
  const GAS u64* p = base + (lane & mask);
#pragma unroll 1
  for (u32 spins = 0; spins < SPIN_CAP; ++spins) {
    u64 v = __hip_atomic_load(p, RLX_AGENT);
    if (__all(v == tag)) break;
    __builtin_amdgcn_s_sleep(1);
  }
  asm volatile("" ::: "memory");
}
#define STAMP(i) do {} while (0)

extern "C" __global__ __launch_bounds__(512, 2) void gqa_block_fused(Params P) {
  extern __shared__ __attribute__((aligned(16))) char lds_[];
  const int tid = threadIdx.x, lane = tid & 63;
  const int wave = __builtin_amdgcn_readfirstlane(tid >> 6);
  const int bid = blockIdx.x;
  const int cidx = (bid & 7) * 4 + (bid >> 6), b = (bid >> 3) & 7;
  const int pi = cidx >> 4, kvh = (cidx >> 1) & 7, sp = cidx & 1;
  const int g = pi * 8 + b, si = kvh * 2 + sp, wg = g * 16 + si;
  const u32 L0 = (u32)(size_t)lds_;
  char* ws = (char*)P.ws;
  const int h = lane >> 5, r = lane & 31, blk = (lane >> 4) & 1, q4 = (lane >> 2) & 3, p4 = lane & 3;
  const __amdgpu_buffer_rsrc_t sW = __builtin_amdgcn_make_buffer_rsrc(ws, 0, (int)WS_END, 0x00020000);
  STAMP(0);

  if (wave >= 4) {
    const int w = wave - 4;
    const __amdgpu_buffer_rsrc_t sX = __builtin_amdgcn_make_buffer_rsrc((char*)P.x[pi] + (size_t)b * 32768, 0, 32768, 0x00020000);
    const __amdgpu_buffer_rsrc_t sQ = __builtin_amdgcn_make_buffer_rsrc((char*)P.wq[pi] + (size_t)(kvh * 512 + sp * 256) * 2, 0, 511 * 8192 + 512, 0x00020000);
    const __amdgpu_buffer_rsrc_t sO = __builtin_amdgcn_make_buffer_rsrc((char*)P.wo[pi] + (size_t)(kvh * 4 + sp * 2) * 128 * 1024, 0, 262144, 0x00020000);
    const size_t kvo = ((size_t)(b * 8 + kvh) * 2048 + (size_t)sp * 1024) * 256;
    const __amdgpu_buffer_rsrc_t sKV = __builtin_amdgcn_make_buffer_rsrc((char*)(w < 2 ? P.k[pi] : P.v[pi]) + kvo, 0, 262144, 0x00020000);
    u32 vq[2], vo[2], vk[2];
#pragma unroll
    for (int e = 0; e < 2; ++e) {
      const u32 rm = (2 * e + h) & 3, ch = (u32)r ^ (rm << 2);
      vq[e] = (u32)h * 8192u + 16u * ch;
      vo[e] = (u32)h * 1024u + 256u * (ch >> 3) + 16u * (ch & 7u) + 128u;
    }
#pragma unroll
    for (int i = 0; i < 2; ++i) vk[i] = 256u * (u32)((lane >> 2) & 7) + 64u * (u32)h + 16u * ((u32)(lane & 3) ^ (u32)(2 * i + ((lane >> 4) & 1)));
    const u32 vw = (u32)(lane >> 3) * 1024u + 16u * ((u32)(lane & 7) ^ (u32)(((lane >> 4) & 1) << 2));

    auto issue_x = [&]() {
#pragma unroll
      for (int i = 0; i < 8; ++i) { const u32 q = 8u * w + i; dma(L0 + RING + 1024u * q, (16u * lane) ^ (16u * (q & 15u)), sX, 1024u * q); }
    };
    auto issue_wq = [&](int step) {
      const u32 c = (u32)(step - 1 + b) & 7u, buf = L0 + RING + (u32)(step & 3) * BUFB;
#pragma unroll
      for (int i = 0; i < 8; ++i) { const u32 I = 8u * w + i; dma(buf + 1024u * I, vq[i & 1], sQ, (64u * c + 2u * I) * 8192u); }
    };
    auto issue_wol = [&](int step) {
      const u32 c = (u32)(step - 25 + b) & 3u, buf = L0 + RING + (u32)(step & 3) * BUFB;
#pragma unroll
      for (int i = 0; i < 8; ++i) { const u32 I = 8u * w + i; dma(buf + 1024u * I, vo[i & 1], sO, (64u * c + 2u * I) * 1024u); }
    };
    auto issue_w = [&](int cc) {
      const u32 c = (u32)(cc + b) & 3u;
#pragma unroll
      for (int i = 0; i < 8; ++i) dma(L0 + SCR + 8192u * w + 1024u * i, vw, sO, (64u * c + 8u * i) * 1024u + 256u * w);
    };
    auto issue_kv = [&](int step) {
      const u32 t = step - 9, buf = L0 + RING + (u32)(step & 3) * BUFB + 8192u * w, so = t * 16384u + 8192u * (w & 1);
#pragma unroll
      for (int i = 0; i < 8; ++i) dma(buf + 1024u * i, vk[(i >> 1) & 1], sKV, so + 2048u * (i >> 1) + 128u * (i & 1));
    };
    auto issue = [&](int step) {
      if (step == 0) issue_x();
      else if (step <= 8) issue_wq(step);
      else if (step <= 24) issue_kv(step);
      else issue_wol(step);
    };
    bf16x8 wr[2][16];
    const u32 aw0 = L0 + SCR + 8192u * w + 128u * (4u * h + q4) + 16u * ((2u * blk + (p4 >> 1)) ^ (u32)((q4 >> 1) << 2)) + 8u * (p4 & 1);
    auto read_w = [&](int cc) {
#pragma unroll
      for (int kk = 0; kk < 4; ++kk)
#pragma unroll
        for (int T = 0; T < 2; ++T) { const u32 a = (aw0 ^ (64u * T)) + 2048u * kk; wr[T][4 * cc + kk] = trfrag(a, a + 1024u); }
      asm volatile("s_waitcnt lgkmcnt(0)" ::: "memory");
    };
    issue(0); issue(1); issue(2); issue(3);
    waitvm<24>(); BAR();
#pragma unroll
    for (int i = 1; i <= 8; ++i) { waitvm<16>(); BAR(); issue(i + 3); }
    BAR(); issue(12);
    waitvm<24>(); BAR(); issue_w(0);
#pragma unroll
    for (int c = 0; c < 4; ++c) {
      waitvm<24>(); BAR(); issue(13 + 2 * c);
      waitvm<24>(); BAR(); issue(14 + 2 * c);
      waitvm<16>(); read_w(c);
      if (c < 3) issue_w(c + 1);
    }
    for (int i = 18; i <= 21; ++i) { waitvm<16>(); BAR(); issue_kv(i + 3); }
    waitvm<16>(); BAR(); issue(25);
    waitvm<16>(); BAR(); issue(26);
    waitvm<16>(); BAR(); issue(27);
    BAR(); issue(28);
    STAMP(5);
    DRAIN(); BAR();
    STAMP(6);
    const u64 tag = *(volatile LAS u64*)(size_t)(L0 + MISC);
    f32x16 acc[2] = {f32x16{}, f32x16{}};
    {
      bf16x8 B[16];
#pragma unroll
      for (int j = 0; j < 16; ++j) B[j] = lds128(L0 + SCR + 1024u * (4u * ((u32)((j >> 2) + b) & 3u) + (j & 3)) + 16u * lane);
#pragma unroll
      for (int j = 0; j < 16; ++j) { acc[0] = MFMA(wr[0][j], B[j], acc[0]); acc[1] = MFMA(wr[1][j], B[j], acc[1]); }
    }
#pragma unroll
    for (int n = 0; n < 2; ++n)
#pragma unroll
      for (int u = 0; u < 2; ++u)
        __builtin_amdgcn_raw_buffer_store_b128(__builtin_bit_cast(u32x4, pack8(acc[n], u)), sW, 1024u * u + 16u * lane, WS_SL + (u32)((g * 16 + 4 * w + n) * 16 + si) * 2048u, AUX_SC1);
    DRAIN();
    if (lane == 0) __hip_atomic_store((GAS u64*)(ws + WS_F3 + (g * 8 + wave) * 128) + si, tag, RLX_AGENT);
    STAMP(7);
    return;
  }

  const int w = wave;
  const u64 tag = __hip_atomic_load((GAS u64*)(ws + WS_EP + g * 256), RLX_AGENT) + TAGC;
  BAR();
  STAMP(1);
  bf16x8 qf[8];
  {
    bf16x8 xf[32];
#pragma unroll
    for (int j = 0; j < 32; ++j) {
      const u32 ks = 4u * ((u32)((j >> 2) + b) & 7u) + (j & 3);
      xf[j] = lds128(L0 + RING + 1024u * r + ((32u * ks + 16u * h) ^ (16u * (r & 15))));
    }
    f32x16 qa[2] = {f32x16{}, f32x16{}};
    const u32 aq0 = 512u * (8u * h + q4) + 16u * ((8u * w + 2u * blk + (p4 & 1)) ^ (4u * q4)) + 8u * (p4 >> 1);
#pragma unroll
    for (int c = 0; c < 8; ++c) {
      BAR();
      const u32 buf = L0 + RING + (u32)((c + 1) & 3) * BUFB;
      bf16x8 af[8];
#pragma unroll
      for (int kk = 0; kk < 4; ++kk)
#pragma unroll
        for (int T = 0; T < 2; ++T) { const u32 a = buf + (aq0 ^ (64u * T)) + 8192u * kk; af[2 * kk + T] = trfrag(a, a + 2048u); }
      SBAR();
#pragma unroll
      for (int kk = 0; kk < 4; ++kk)
#pragma unroll
        for (int T = 0; T < 2; ++T) qa[T] = MFMA(af[2 * kk + T], xf[4 * c + kk], qa[T]);
    }
    STAMP(2);
    const u32 fo = (u32)((w >> 1) * 8 + 4 * (w & 1)) * 1024u + 16u * lane;
#pragma unroll
    for (int T = 0; T < 2; ++T)
#pragma unroll
      for (int s = 0; s < 2; ++s) {
        const bf16x8 f = pack8(qa[T], s);
        lds_st128(L0 + SCR + fo + 1024u * (2 * T + s), f);
        __builtin_amdgcn_raw_buffer_store_b128(__builtin_bit_cast(u32x4, f), sW, fo + 1024u * (2 * T + s), WS_QX + (u32)wg * 16384u, AUX_SC1);
      }
  }
  const bool own = (w >> 1) == sp;
  const int hl = w & 1;
  BAR();
  if (own) {
#pragma unroll
    for (int ks = 0; ks < 8; ++ks) qf[ks] = lds128(L0 + SCR + (u32)(hl * 8 + ks) * 1024u + 16u * lane);
  }
  DRAIN();
  if (lane == 0) __hip_atomic_store((GAS u64*)(ws + WS_FQ + wg * 128) + w, tag, RLX_AGENT);
  if (!own) {
    poll_flags((const GAS u64*)(ws + WS_FQ + (wg ^ 1) * 128) + 2 * hl, 1, lane, tag);
#pragma unroll
    for (int ks = 0; ks < 8; ++ks)
      qf[ks] = __builtin_bit_cast(bf16x8, __builtin_amdgcn_raw_buffer_load_b128(sW, (u32)(hl * 8 + ks) * 1024u + 16u * lane, WS_QX + (u32)(wg ^ 1) * 16384u, AUX_SC1));
    DRAIN();
  }
  STAMP(3);
  f32x16 o[4];
#pragma unroll
  for (int d = 0; d < 4; ++d) o[d] = f32x16{};
  float mc = -__builtin_inff();
  f32x2 l2 = {0.f, 0.f};
  u32 akb[2], avb[2];
#pragma unroll
  for (int e = 0; e < 2; ++e) {
    akb[e] = 2048u * (u32)(r >> 3) + 64u * (u32)(r & 7) + 16u * ((u32)(2 * e + h) ^ (u32)((r >> 2) & 3));
    avb[e] = 16384u + 2048u * e + 64u * (u32)(4 * h + q4) + 16u * ((u32)(2 * blk + (p4 >> 1)) ^ (u32)((2 * e + h) & 3)) + 8u * (u32)(p4 & 1);
  }
  auto ldk = [&](bf16x8 (&kf)[8], u32 buf, int kb) {
    u32 a0 = buf + akb[0], a1 = buf + akb[1];
    asm("" : "+v"(a0)); asm("" : "+v"(a1));
#pragma unroll
    for (int ks = 0; ks < 8; ++ks) kf[ks] = lds128(((ks & 1) ? a1 : a0) + 8192u * kb + 512u * (ks >> 1));
  };
  auto ldv = [&](bf16x8 (&vf)[8], u32 buf, int kb) {
    const u32 a0 = buf + avb[0], a1 = buf + avb[1];
#pragma unroll
    for (int s = 0; s < 2; ++s)
#pragma unroll
      for (int d = 0; d < 4; ++d) vf[4 * s + d] = trfrag(a0 + 8192u * kb + 4096u * s + 512u * d, a1 + 8192u * kb + 4096u * s + 512u * d);
  };
  auto rowmax = [&](const f32x16& s) {
    float m0 = fmaxf(s[0], s[1]);
    float ma = max3(m0, s[2], s[3]), mb = max3(m0, s[4], s[5]);
    ma = max3(ma, s[6], s[7]); mb = max3(mb, s[8], s[9]);
    ma = max3(ma, s[10], s[11]); mb = max3(mb, s[12], s[13]);
    ma = max3(ma, s[14], s[15]);
    return swap_max(fmaxf(ma, mb)) * CSC;
  };
  auto exp4 = [&](f32x16& s, int i) {
#pragma unroll
    for (int e = 0; e < 4; ++e) s[4 * i + e] = __builtin_amdgcn_exp2f(fmaf(s[4 * i + e], CSC, -mc));
    l2 += f32x2{s[4 * i], s[4 * i + 1]}; l2 += f32x2{s[4 * i + 2], s[4 * i + 3]};
  };
  bf16x8 kf0[8], kf1[8], vf0[8], vf1[8];
  BAR();
  ldk(kf0, L0 + RING + (u32)(9 & 3) * BUFB, 0);
  for (int t = 0; t < 16; ++t) {
    const u32 buf = L0 + RING + (u32)((t + 9) & 3) * BUFB;
    f32x16 s0 = f32x16{}, s1 = f32x16{};
    ldk(kf1, buf, 1);
    SBAR();
#pragma unroll
    for (int ks = 0; ks < 8; ++ks) s0 = MFMA(kf0[ks], qf[ks], s0);
    PIN(s0);
    SBAR();
    ldv(vf0, buf, 0);
    SBAR();
    s1 = MFMA(kf1[0], qf[0], s1); PIN(s1); SBAR();
    s1 = MFMA(kf1[1], qf[1], s1); PIN(s1); SBAR();
    {
      const float mx = rowmax(s0);
      s1 = MFMA(kf1[2], qf[2], s1); PIN(s1);
      if (__any(mx - mc > THR)) {
        const float mn = fmaxf(mc, mx);
        const float al = __builtin_amdgcn_exp2f(mc - mn);
        l2 *= al; mc = mn;
#pragma unroll
        for (int d = 0; d < 4; ++d)
#pragma unroll
          for (int i = 0; i < 16; ++i) o[d][i] *= al;
      }
    }
    bf16x8 p0[2], p1[2];
    s1 = MFMA(kf1[3], qf[3], s1); PIN(s1); SBAR(); exp4(s0, 0); PIN(s0); SBAR();
    s1 = MFMA(kf1[4], qf[4], s1); PIN(s1); SBAR(); exp4(s0, 1); p0[0] = pack8(s0, 0); PIN(p0[0]); PIN(s0); SBAR();
    s1 = MFMA(kf1[5], qf[5], s1); PIN(s1); SBAR(); exp4(s0, 2); PIN(s0); SBAR();
    s1 = MFMA(kf1[6], qf[6], s1); PIN(s1); SBAR(); exp4(s0, 3); p0[1] = pack8(s0, 1); PIN(p0[1]); PIN(l2); SBAR();
    s1 = MFMA(kf1[7], qf[7], s1); PIN(s1); SBAR();
    ldv(vf1, buf, 1);
    SBAR();
    o[0] = MFMA(vf0[0], p0[0], o[0]); PIN(o[0]); SBAR();
    o[1] = MFMA(vf0[1], p0[0], o[1]); PIN(o[1]); SBAR();
    float al1 = 1.f; bool pend = false;
    {
      const float mx = rowmax(s1);
      o[2] = MFMA(vf0[2], p0[0], o[2]); PIN(o[2]);
      if (__any(mx - mc > THR)) {
        const float mn = fmaxf(mc, mx);
        al1 = __builtin_amdgcn_exp2f(mc - mn);
        l2 *= al1; mc = mn; pend = true;
      }
    }
    o[3] = MFMA(vf0[3], p0[0], o[3]); PIN(o[3]); SBAR(); exp4(s1, 0); PIN(s1); SBAR();
    o[0] = MFMA(vf0[4], p0[1], o[0]); PIN(o[0]); SBAR(); exp4(s1, 1); p1[0] = pack8(s1, 0); PIN(p1[0]); PIN(s1); SBAR();
    o[1] = MFMA(vf0[5], p0[1], o[1]); PIN(o[1]); SBAR(); exp4(s1, 2); PIN(s1); SBAR();
    o[2] = MFMA(vf0[6], p0[1], o[2]); PIN(o[2]); SBAR(); exp4(s1, 3); p1[1] = pack8(s1, 1); PIN(p1[1]); PIN(l2); SBAR();
    o[3] = MFMA(vf0[7], p0[1], o[3]); PIN(o[3]); SBAR();
    if (pend) {
#pragma unroll
      for (int d = 0; d < 4; ++d)
#pragma unroll
        for (int i = 0; i < 16; ++i) o[d][i] *= al1;
    }
    BAR();
    if (t < 15) ldk(kf0, L0 + RING + (u32)((t + 10) & 3) * BUFB, 0);
    SBAR();
#pragma unroll
    for (int s = 0; s < 2; ++s)
#pragma unroll
      for (int d = 0; d < 4; ++d) o[d] = MFMA(vf1[4 * s + d], p1[s], o[d]);
  }
  const float l = swap_add(l2[0] + l2[1]);
  STAMP(4);
  if (!own) {
    const u32 ob = WS_OX + (u32)(wg * 2 + hl) * OXH;
#pragma unroll
    for (int d = 0; d < 4; ++d)
#pragma unroll
      for (int u = 0; u < 4; ++u) {
        f32x4 v = {o[d][4 * u], o[d][4 * u + 1], o[d][4 * u + 2], o[d][4 * u + 3]};
        __builtin_amdgcn_raw_buffer_store_b128(__builtin_bit_cast(u32x4, v), sW, 1024u * (4 * d + u) + 16u * lane, ob, AUX_SC1);
      }
    if (lane < 32) { u32x2 st = {__float_as_uint(mc), __float_as_uint(l)}; __builtin_amdgcn_raw_buffer_store_b64(st, sW, 16384u + 8u * lane, ob, AUX_SC1); }
    DRAIN();
    if (lane == 0) __hip_atomic_store((GAS u64*)(ws + WS_FO + wg * 128) + hl, tag, RLX_AGENT);
    if (w == 2 * (sp ^ 1) && lane == 0) *(volatile LAS u64*)(size_t)(L0 + MISC) = tag;
  } else {
    poll_flags((const GAS u64*)(ws + WS_FO + (wg ^ 1) * 128) + hl, 0, lane, tag);
    const u32 ob = WS_OX + (u32)((wg ^ 1) * 2 + hl) * OXH;
    const u32x2 st = __builtin_amdgcn_raw_buffer_load_b64(sW, 16384u + 8u * r, ob, AUX_SC1);
    f32x4 pv[16];
#pragma unroll
    for (int i = 0; i < 16; ++i) pv[i] = __builtin_bit_cast(f32x4, __builtin_amdgcn_raw_buffer_load_b128(sW, 1024u * i + 16u * lane, ob, AUX_SC1));
    SBAR();
    const float m2 = __uint_as_float(st[0]), lp = __uint_as_float(st[1]);
    const float mn = fmaxf(mc, m2);
    const float a1 = __builtin_amdgcn_exp2f(mc - mn), a2 = __builtin_amdgcn_exp2f(m2 - mn);
    const float inv = 1.0f / (l * a1 + lp * a2);
    const float f1 = a1 * inv, f2 = a2 * inv;
#pragma unroll
    for (int d = 0; d < 4; ++d) {
#pragma unroll
      for (int u = 0; u < 4; ++u)
#pragma unroll
        for (int e = 0; e < 4; ++e) o[d][4 * u + e] = o[d][4 * u + e] * f1 + pv[4 * d + u][e] * f2;
      lds_st128(L0 + SCR + (u32)(hl * 8 + 2 * d) * 1024u + 16u * lane, pack8(o[d], 0));
      lds_st128(L0 + SCR + (u32)(hl * 8 + 2 * d + 1) * 1024u + 16u * lane, pack8(o[d], 1));
    }
  }
  STAMP(5);
  BAR();
  STAMP(6);
  {
    f32x16 acc[2] = {f32x16{}, f32x16{}};
    const u32 ao0 = 512u * (4u * h + q4) + 16u * ((8u * w + 2u * blk + (p4 >> 1)) ^ (4u * q4)) + 8u * (p4 & 1);
#pragma unroll
    for (int c = 0; c < 4; ++c) {
      bf16x8 B[4], A[8];
      const u32 buf = L0 + RING + (u32)((25 + c) & 3) * BUFB;
#pragma unroll
      for (int kk = 0; kk < 4; ++kk) {
        B[kk] = lds128(L0 + SCR + 1024u * (4u * ((u32)(c + b) & 3u) + kk) + 16u * lane);
#pragma unroll
        for (int T = 0; T < 2; ++T) { const u32 a = buf + (ao0 ^ (64u * T)) + 8192u * kk; A[2 * kk + T] = trfrag(a, a + 4096u); }
      }
#pragma unroll
      for (int kk = 0; kk < 4; ++kk) { acc[0] = MFMA(A[2 * kk], B[kk], acc[0]); acc[1] = MFMA(A[2 * kk + 1], B[kk], acc[1]); }
    }
#pragma unroll
    for (int n = 0; n < 2; ++n)
#pragma unroll
      for (int u = 0; u < 2; ++u)
        __builtin_amdgcn_raw_buffer_store_b128(__builtin_bit_cast(u32x4, pack8(acc[n], u)), sW, 1024u * u + 16u * lane, WS_SL + (u32)((g * 16 + 4 * w + 2 + n) * 16 + si) * 2048u, AUX_SC1);
    DRAIN();
    if (lane == 0) __hip_atomic_store((GAS u64*)(ws + WS_F3 + (g * 8 + wave) * 128) + si, tag, RLX_AGENT);
  }
  STAMP(7);
  poll_flags((const GAS u64*)(ws + WS_F3 + (g * 8 + (si >> 2) + ((si & 2) ? 0 : 4)) * 128), 15, lane, tag);
  f32x4 sum = {0.f, 0.f, 0.f, 0.f};
  {
    u32x2 v[16];
#pragma unroll
    for (int i = 0; i < 16; ++i)
      v[i] = __builtin_amdgcn_raw_buffer_load_b64(sW, 1024u * (w >> 1) + 16u * lane + 8u * (w & 1) + 2048u * i, WS_SL + (u32)((g * 16 + si) * 16) * 2048u, AUX_SC1);
#pragma unroll
    for (int i = 0; i < 16; ++i) {
      sum[0] += __uint_as_float(v[i][0] << 16); sum[1] += __uint_as_float(v[i][0] & 0xffff0000u);
      sum[2] += __uint_as_float(v[i][1] << 16); sum[3] += __uint_as_float(v[i][1] & 0xffff0000u);
    }
  }
  u32x2 ov = {cvtpk(sum[0], sum[1]), cvtpk(sum[2], sum[3])};
  *(GAS u32x2*)((char*)P.out + ((size_t)(b * 32 + r) * 1024 + pi * 512 + 32 * si + 8 * w + 4 * h) * 2) = ov;
  if (w == 0 && lane == 0) __hip_atomic_store((GAS u64*)(ws + WS_EP + g * 256), tag, RLX_AGENT);
  STAMP(8);
}

extern "C" void kernel_launch(void* const* d_in, const int* in_sizes, int n_in, void* d_out, int out_size, void* d_ws, size_t ws_size, hipStream_t stream) {
  (void)hipFuncSetAttribute((const void*)gqa_block_fused, hipFuncAttributeMaxDynamicSharedMemorySize, (int)LDS_BYTES);
  Params P;
  for (int p = 0; p < 2; ++p) { P.x[p] = d_in[5 * p + 0]; P.k[p] = d_in[5 * p + 1]; P.v[p] = d_in[5 * p + 2]; P.wq[p] = d_in[5 * p + 3]; P.wo[p] = d_in[5 * p + 4]; }
  P.out = d_out; P.ws = d_ws;
  hipLaunchKernelGGL(gqa_block_fused, dim3(256), dim3(512), LDS_BYTES, stream, P);
}
```

```cpp
#include <hip/hip_runtime.h>
#include <stdint.h>
#include <stdio.h>
#include <type_traits>

typedef unsigned u32;
typedef unsigned long long u64;
typedef u32 u32x4 __attribute__((ext_vector_type(4)));
typedef u32 u32x2 __attribute__((ext_vector_type(2)));
typedef float f32x2 __attribute__((ext_vector_type(2)));
typedef float f32x4 __attribute__((ext_vector_type(4)));
typedef float f32x16 __attribute__((ext_vector_type(16)));
typedef __bf16 bf16x2 __attribute__((ext_vector_type(2)));
typedef __bf16 bf16x4 __attribute__((ext_vector_type(4)));
typedef __bf16 bf16x8 __attribute__((ext_vector_type(8)));
#define LAS __attribute__((address_space(3)))
#define GAS __attribute__((address_space(1)))
#define DI __device__ __forceinline__
#define RLX_AGENT __ATOMIC_RELAXED, __HIP_MEMORY_SCOPE_AGENT
#define SBAR() __builtin_amdgcn_sched_barrier(0)
#define PIN(x) asm volatile("" : "+v"(x))

constexpr u32 KR = 0, VR = 65536, TB = 16384, EX = 131072, LDS_BYTES = 163840;
constexpr u32 MS = 98304;
constexpr u32 MISC = MS + 6144;
constexpr int NT = 32, NREG = 6;
constexpr u32 WS_EP = 0;
constexpr u32 WS_F3 = 4096;
constexpr u32 WS_SL = 32768;
constexpr u32 WS_END = WS_SL + 16u * 16u * 16u * 2048u;
constexpr u32 WS_DBG = WS_END;
constexpr u64 TAGC = 0x9E3779B97F4A7C15ull;
constexpr float CSC = 0.088388347648318447f * 1.4426950408889634f;
constexpr float THR = 8.0f;
constexpr int AUX_SC1 = 16, AUX_NT = 0;
constexpr u32 SPIN_CAP = 1u << 18;

struct Params {
  const void* x[2]; const void* k[2]; const void* v[2]; const void* wq[2]; const void* wo[2];
  void* out; void* ws;
};

#define BAR() asm volatile("s_waitcnt lgkmcnt(0)\n\ts_barrier" ::: "memory")
#define DRAIN() asm volatile("s_waitcnt vmcnt(0)" ::: "memory")
#define LGKM0() asm volatile("s_waitcnt lgkmcnt(0)" ::: "memory")
template <int N> DI void waitvm() { asm volatile("s_waitcnt vmcnt(%0)" ::"i"(N) : "memory"); }

DI void dma(u32 m0v, u32 voff, __amdgpu_buffer_rsrc_t srd, u32 soff) {
  asm volatile("s_mov_b32 m0, %0\n\ts_nop 0\n\tbuffer_load_dwordx4 %1, %2, %3 offen lds" ::"s"(m0v), "v"(voff), "s"(srd), "s"(soff) : "memory");
}
DI u32 cvtpk(float lo, float hi) { f32x2 v = {lo, hi}; bf16x2 b = __builtin_convertvector(v, bf16x2); return __builtin_bit_cast(u32, b); }
DI bf16x8 pack8(const f32x16& x, int s) {
  u32x4 w = {cvtpk(x[8 * s], x[8 * s + 1]), cvtpk(x[8 * s + 2], x[8 * s + 3]), cvtpk(x[8 * s + 4], x[8 * s + 5]), cvtpk(x[8 * s + 6], x[8 * s + 7])};
  return __builtin_bit_cast(bf16x8, w);
}
DI bf16x8 lds128(u32 a) { return *(const LAS bf16x8*)(size_t)a; }
DI u32x4 lds128u(u32 a) { return *(const LAS u32x4*)(size_t)a; }
DI void lds_st128(u32 a, bf16x8 v) { *(LAS bf16x8*)(size_t)a = v; }
DI void lds_st128u(u32 a, u32x4 v) { *(LAS u32x4*)(size_t)a = v; }
DI bf16x8 trfrag(u32 a0, u32 a1) {
  bf16x4 lo = __builtin_amdgcn_ds_read_tr16_b64_v4bf16((LAS bf16x4*)(size_t)a0);
  bf16x4 hi = __builtin_amdgcn_ds_read_tr16_b64_v4bf16((LAS bf16x4*)(size_t)a1);
  return __builtin_shufflevector(lo, hi, 0, 1, 2, 3, 4, 5, 6, 7);
}
#define MFMA(a, b, c) __builtin_amdgcn_mfma_f32_32x32x16_bf16((a), (b), (c), 0, 0, 0)
DI float swap_max(float v) { auto r = __builtin_amdgcn_permlane32_swap(__float_as_uint(v), __float_as_uint(v), false, false); return fmaxf(__uint_as_float(r[0]), __uint_as_float(r[1])); }
DI float swap_add(float v) { auto r = __builtin_amdgcn_permlane32_swap(__float_as_uint(v), __float_as_uint(v), false, false); return __uint_as_float(r[0]) + __uint_as_float(r[1]); }
DI float max3(float a, float b, float c) { float r; asm("v_max3_f32 %0, %1, %2, %3" : "=v"(r) : "v"(a), "v"(b), "v"(c)); return r; }

DI void poll_flags(const GAS u64* base, int mask, int lane, u64 tag) {
  const GAS u64* p = base + (lane & mask);
#pragma unroll 1
  for (u32 spins = 0; spins < SPIN_CAP; ++spins) {
    u64 v = __hip_atomic_load(p, RLX_AGENT);
    if (__all(v == tag)) break;
    __builtin_amdgcn_s_sleep(1);
  }
  asm volatile("" ::: "memory");
}
#define STAMP(i) do {} while (0)

struct St {
  bf16x8 qf[8], kf[8], vf[8];
  f32x16 o[4], sA, sB;
  u32x4 p[2];
  float e[2][2];
  float bs[2];
  float l, mc, mx, al;
  bool pend;
  u32 ak[2], av[2];
};
template <int G> DI void sm_slice(St& c, const f32x16& s) {
  c.e[(G >> 1) & 1][G & 1] = __builtin_amdgcn_exp2f(fmaf(s[G], CSC, -c.mc));
  if constexpr (G >= 1) { constexpr int H = G - 1; c.bs[H & 1] += c.e[(H >> 1) & 1][H & 1]; PIN(c.bs[H & 1]); }
  if constexpr (G >= 2 && (G & 1) == 0) { constexpr int Q = (G - 2) >> 1; u32 w = cvtpk(c.e[Q & 1][0], c.e[Q & 1][1]); PIN(w); c.p[Q >> 2][Q & 3] = w; }
}
DI void sm_tail(St& c) {
  c.bs[1] += c.e[1][1];
  c.p[1][3] = cvtpk(c.e[1][0], c.e[1][1]);
  c.l += c.bs[0] + c.bs[1];
  c.bs[0] = 0.f; c.bs[1] = 0.f;
}
template <int I> DI bf16x8 ldk1(u32 a0, u32 a1) { return lds128(((I & 1) ? a1 : a0) + 512u * (I >> 1)); }
template <int I> DI bf16x8 ldv1(u32 a0, u32 a1) { return trfrag(a0 + 4096u * (I >> 2) + 512u * (I & 3), a1 + 4096u * (I >> 2) + 512u * (I & 3)); }
template <int I, bool QK> DI void qk_gap(St& c, f32x16& snew, const f32x16& scur, u32 v0, u32 v1) {
  if constexpr (QK) { if constexpr (I == 0) snew = MFMA(c.kf[0], c.qf[0], f32x16{}); else snew = MFMA(c.kf[I], c.qf[I], snew); PIN(snew); }
  c.vf[I] = ldv1<I>(v0, v1);
  if constexpr (I < 4) { sm_slice<4 + 2 * I>(c, scur); sm_slice<5 + 2 * I>(c, scur); }
  else sm_slice<8 + I>(c, scur);
  if constexpr (I == 7) sm_tail(c);
  SBAR();
}
template <int I, bool MX> DI void pv_gap(St& c, const f32x16& s) {
  c.o[I & 3] = MFMA(c.vf[I], __builtin_bit_cast(bf16x8, c.p[I >> 2]), c.o[I & 3]); PIN(c.o[I & 3]);
  if constexpr (MX) {
    if constexpr (I == 2) {
      const float m0 = fmaxf(s[0], s[1]);
      float ma = max3(m0, s[2], s[3]), mb = max3(m0, s[4], s[5]);
      ma = max3(ma, s[6], s[7]); mb = max3(mb, s[8], s[9]);
      c.mx = ma; c.al = mb;
    } else if constexpr (I == 3) {
      float ma = max3(c.mx, s[10], s[11]), mb = max3(c.al, s[12], s[13]);
      ma = max3(ma, s[14], s[15]);
      c.mx = fmaxf(ma, mb);
    } else if constexpr (I == 4) {
      c.pend = false;
      if (__builtin_expect(__any(fmaf(c.mx, CSC, -c.mc) > THR), 0)) {
        const float mn = fmaxf(c.mc, swap_max(c.mx) * CSC);
        c.al = __builtin_amdgcn_exp2f(c.mc - mn);
        c.l *= c.al; c.mc = mn; c.pend = true;
      }
    } else if constexpr (I == 7) {
      if (__builtin_expect(c.pend, 0)) {
#pragma unroll
        for (int d = 0; d < 4; ++d)
#pragma unroll
          for (int i = 0; i < 16; ++i) c.o[d][i] *= c.al;
      }
    }
  }
  SBAR();
}
#define G8(F, ...) F<0 __VA_ARGS__; F<1 __VA_ARGS__; F<2 __VA_ARGS__; F<3 __VA_ARGS__; F<4 __VA_ARGS__; F<5 __VA_ARGS__; F<6 __VA_ARGS__; F<7 __VA_ARGS__;
template <bool NEXT> DI void tile_step(St& c, f32x16& scur, f32x16& snew, u32 L0, int t) {
  u32 k0 = L0 + KR + (u32)((t + 1) & 3) * TB + c.ak[0], k1 = L0 + KR + (u32)((t + 1) & 3) * TB + c.ak[1];
  u32 v0 = L0 + VR + (u32)(t & 3) * TB + c.av[0], v1 = L0 + VR + (u32)(t & 3) * TB + c.av[1];
  asm("" : "+v"(k0)); asm("" : "+v"(k1)); asm("" : "+v"(v0)); asm("" : "+v"(v1));
  BAR();
  if constexpr (NEXT) {
    c.kf[0] = ldk1<0>(k0, k1); c.kf[1] = ldk1<1>(k0, k1); c.kf[2] = ldk1<2>(k0, k1); c.kf[3] = ldk1<3>(k0, k1);
    c.kf[4] = ldk1<4>(k0, k1); c.kf[5] = ldk1<5>(k0, k1); c.kf[6] = ldk1<6>(k0, k1); c.kf[7] = ldk1<7>(k0, k1);
  }
  SBAR();
  sm_slice<0>(c, scur); sm_slice<1>(c, scur); sm_slice<2>(c, scur); sm_slice<3>(c, scur);
  SBAR();
  G8(qk_gap, , NEXT>(c, snew, scur, v0, v1))
  if constexpr (!NEXT) BAR();
  G8(pv_gap, , NEXT>(c, snew))
}

extern "C" __global__ __launch_bounds__(512, 2) void gqa_block_fused(Params P) {
  extern __shared__ __attribute__((aligned(16))) char lds_[];
  const int tid = threadIdx.x, lane = tid & 63;
  const int wave = __builtin_amdgcn_readfirstlane(tid >> 6);
  const int bid = blockIdx.x;
  const int cidx = (bid & 7) * 4 + (bid >> 6), b = (bid >> 3) & 7;
  const int pi = cidx >> 4, kvh = (cidx >> 1) & 7, hh = cidx & 1;
  const int g = pi * 8 + b, si = kvh * 2 + hh;
  const u32 L0 = (u32)(size_t)lds_;
  char* ws = (char*)P.ws;
  const int h = lane >> 5, r = lane & 31, blk = (lane >> 4) & 1, q4 = (lane >> 2) & 3, p4 = lane & 3;
  const __amdgpu_buffer_rsrc_t sW = __builtin_amdgcn_make_buffer_rsrc(ws, 0, (int)WS_END, 0x00020000);
  auto ofrag = [&](u32 ks) -> u32 { const u32 dt = (ks >> 1) & 3u, wo_ = (ks >> 3) + 2u * (dt >> 1); return L0 + MS + 8192u * (wo_ ^ 2u) + 1024u * (2u * (dt & 1u) + (ks & 1u)) + 16u * lane; };
  STAMP(0);

  if (wave >= 4) {
    const int w = wave - 4;
    const size_t kvo = (size_t)(b * 8 + kvh) * 2048 * 256;
    const __amdgpu_buffer_rsrc_t sKV = __builtin_amdgcn_make_buffer_rsrc((char*)(w < 2 ? P.k[pi] : P.v[pi]) + kvo, 0, 524288, 0x00020000);
    const __amdgpu_buffer_rsrc_t sO = __builtin_amdgcn_make_buffer_rsrc((char*)P.wo[pi] + (size_t)(kvh * 4 + hh * 2) * 128 * 1024, 0, 262144, 0x00020000);
    u32 vk[2], vo[2];
#pragma unroll
    for (int i = 0; i < 2; ++i) {
      vk[i] = 256u * (u32)((lane >> 2) & 7) + 64u * (u32)h + 16u * ((u32)(lane & 3) ^ (u32)(2 * i + ((lane >> 4) & 1))) + 8192u * (u32)(w & 1);
      const u32 rm = (2 * i + h) & 3, ch = (u32)r ^ (rm << 2);
      vo[i] = (u32)h * 1024u + 256u * (ch >> 3) + 16u * (ch & 7u) + 128u;
    }
    const u32 vw = (u32)(lane >> 3) * 1024u + 16u * ((u32)(lane & 7) ^ (u32)(((lane >> 4) & 1) << 2)) + 256u * (u32)w;
    const u32 rb = L0 + (w < 2 ? KR : VR) + 8192u * (u32)(w & 1);
    u32x4 st[NREG][8];
    auto load_item = [&](int i) {
#pragma unroll
      for (int j = 0; j < 8; ++j) st[i][j] = __builtin_amdgcn_raw_buffer_load_b128(sKV, vk[(j >> 1) & 1] + 2048u * (j >> 1) + 128u * (j & 1), (u32)i * 16384u, AUX_NT);
      SBAR();
    };
    auto write_tile = [&](int i) {
#pragma unroll
      for (int j = 0; j < 8; ++j) lds_st128u(rb + (u32)(i & 3) * TB + 1024u * j + 16u * lane, st[i][j]);
      SBAR();
    };
    auto dma_tile = [&](int u) {
#pragma unroll
      for (int j = 0; j < 8; ++j) dma(rb + (u32)(u & 3) * TB + 1024u * j, vk[(j >> 1) & 1], sKV, (u32)u * 16384u + 2048u * (j >> 1) + 128u * (j & 1));
    };
    bf16x8 wr[2][16];
    const u32 aw0 = L0 + EX + 8192u * w + 128u * (4u * h + q4) + 16u * ((2u * blk + (p4 >> 1)) ^ (u32)((q4 >> 1) << 2)) + 8u * (p4 & 1);
    auto issue_w = [&](int cc) {
      const u32 c = (u32)(cc + b) & 3u;
#pragma unroll
      for (int i = 0; i < 8; ++i) dma(L0 + EX + 8192u * w + 1024u * i, vw, sO, (64u * c + 8u * i) * 1024u);
    };
    auto read_w = [&](int cc) {
#pragma unroll
      for (int kk = 0; kk < 4; ++kk)
#pragma unroll
        for (int T = 0; T < 2; ++T) { const u32 a = (aw0 ^ (64u * T)) + 2048u * kk; wr[T][4 * cc + kk] = trfrag(a, a + 1024u); }
      LGKM0();
      SBAR();
    };
    auto issue_wol = [&](int cc, u32 dst) {
      const u32 c = (u32)(cc + b) & 3u;
#pragma unroll
      for (int i = 0; i < 8; ++i) { const u32 I = 8u * w + i; dma(L0 + dst + 1024u * I, vo[i & 1], sO, (64u * c + 2u * I) * 1024u); }
    };
    BAR();
    load_item(0); load_item(1); load_item(2);
    BAR(); BAR(); BAR();
    load_item(3); load_item(4); load_item(5);
    BAR();
    write_tile(0); write_tile(1); write_tile(2); write_tile(3);
    BAR();
    auto stream = [&](auto offc) {
      constexpr int OFF = decltype(offc)::value;
#pragma unroll
      for (int t = 0; t < NT; ++t) {
        if (t - 3 + OFF >= NREG && t - 3 + OFF < NT) waitvm<16>();
        BAR();
        const int u = t + OFF;
        if (u >= 4 && u < NREG) write_tile(u);
        else if (u >= NREG && u < NT) dma_tile(u);
        if (t == 14 || t == 18 || t == 22 || t == 26) read_w((t - 14) >> 2);
        if (t == 10 || t == 14 || t == 18 || t == 22) issue_w((t - 10) >> 2);
        if (t == 27) issue_wol(3, EX);
        if (t == 29) issue_wol(0, KR);
        if (t == 30) issue_wol(1, VR);
        if (t == 31) issue_wol(2, KR + 2 * TB);
      }
    };
    if (w < 2) stream(std::integral_constant<int, 4>{}); else stream(std::integral_constant<int, 3>{});
    BAR();
    BAR();
    STAMP(5);
    DRAIN(); BAR();
    STAMP(6);
    const u64 tag = *(volatile LAS u64*)(size_t)(L0 + MISC);
    f32x16 acc[2] = {f32x16{}, f32x16{}};
    {
      bf16x8 B[16];
#pragma unroll
      for (int j = 0; j < 16; ++j) B[j] = lds128(ofrag(4u * ((u32)((j >> 2) + b) & 3u) + (j & 3)));
#pragma unroll
      for (int j = 0; j < 16; ++j) { acc[0] = MFMA(wr[0][j], B[j], acc[0]); acc[1] = MFMA(wr[1][j], B[j], acc[1]); }
    }
#pragma unroll
    for (int n = 0; n < 2; ++n)
#pragma unroll
      for (int u = 0; u < 2; ++u)
        __builtin_amdgcn_raw_buffer_store_b128(__builtin_bit_cast(u32x4, pack8(acc[n], u)), sW, 1024u * u + 16u * lane + WS_SL + (u32)((g * 16 + 4 * w + n) * 16 + si) * 2048u, 0, AUX_SC1);
    DRAIN();
    if (lane == 0) __hip_atomic_store((GAS u64*)(ws + WS_F3 + (g * 8 + wave) * 128) + si, tag, RLX_AGENT);
    STAMP(7);
    return;
  }

  const int w = wave;
  St c;
  u64 tag;
  {
    const __amdgpu_buffer_rsrc_t sX = __builtin_amdgcn_make_buffer_rsrc((char*)P.x[pi] + (size_t)b * 32768, 0, 32768, 0x00020000);
    const __amdgpu_buffer_rsrc_t sQ = __builtin_amdgcn_make_buffer_rsrc((char*)P.wq[pi] + (size_t)(kvh * 512 + hh * 256) * 2, 0, 511 * 8192 + 512, 0x00020000);
    const u32 vq = (u32)(lane >> 3) * 8192u + 16u * ((u32)(lane & 7) ^ (u32)(((lane >> 4) & 1) << 2)) + 128u * (u32)w;
    auto issue_wq = [&](int cc) {
      const u32 ch = (u32)(cc + b) & 7u;
#pragma unroll
      for (int i = 0; i < 8; ++i) dma(L0 + 32768u * w + 8192u * (cc & 3) + 1024u * i, vq, sQ, (64u * ch + 8u * i) * 8192u);
    };
    {
      const GAS u64* ep = (const GAS u64*)(ws + WS_EP + g * 256);
      asm volatile("global_load_dwordx2 %0, %1, off sc1" : "=v"(tag) : "v"(ep) : "memory");
    }
#pragma unroll
    for (int i = 0; i < 8; ++i) { const u32 q = 8u * w + i; dma(L0 + EX + 1024u * q, (16u * lane) ^ (16u * (q & 15u)), sX, 1024u * q); }
    issue_wq(0); issue_wq(1); issue_wq(2); issue_wq(3);
    BAR();
    waitvm<32>(); BAR();
    asm volatile("" : "+v"(tag));
    tag += TAGC;
    STAMP(1);
    bf16x8 xf[32];
#pragma unroll
    for (int j = 0; j < 32; ++j) {
      const u32 ks = 4u * ((u32)((j >> 2) + b) & 7u) + (j & 3);
      xf[j] = lds128(L0 + EX + 1024u * r + ((32u * ks + 16u * h) ^ (16u * (r & 15))));
    }
    BAR();
    f32x16 qa[2] = {f32x16{}, f32x16{}};
    const u32 aq0 = L0 + 32768u * w + 128u * (8u * h + q4) + 16u * ((2u * blk + (p4 & 1)) ^ (u32)((q4 >> 1) << 2)) + 8u * (p4 >> 1);
#pragma unroll
    for (int cc = 0; cc < 8; ++cc) {
      if (cc <= 4) waitvm<24>(); else if (cc == 5) waitvm<16>(); else if (cc == 6) waitvm<8>(); else waitvm<0>();
      bf16x8 af[8];
#pragma unroll
      for (int kk = 0; kk < 4; ++kk)
#pragma unroll
        for (int T = 0; T < 2; ++T) { const u32 a = (aq0 ^ (64u * T)) + 8192u * (cc & 3) + 2048u * kk; af[2 * kk + T] = trfrag(a, a + 512u); }
      LGKM0();
      if (cc < 4) issue_wq(cc + 4);
      if (cc == 3) BAR();
      SBAR();
#pragma unroll
      for (int kk = 0; kk < 4; ++kk)
#pragma unroll
        for (int T = 0; T < 2; ++T) qa[T] = MFMA(af[2 * kk + T], xf[4 * cc + kk], qa[T]);
    }
    STAMP(2);
    const u32 fo = (u32)((w >> 1) * 8 + 4 * (w & 1)) * 1024u + 16u * lane;
#pragma unroll
    for (int T = 0; T < 2; ++T)
#pragma unroll
      for (int s = 0; s < 2; ++s) lds_st128(L0 + EX + fo + 1024u * (2 * T + s), pack8(qa[T], s));
  }
  const int hl = w & 1, kbw = w >> 1;
  BAR();
#pragma unroll
  for (int ks = 0; ks < 8; ++ks) c.qf[ks] = lds128(L0 + EX + (u32)(hl * 8 + ks) * 1024u + 16u * lane);
#pragma unroll
  for (int e = 0; e < 2; ++e) {
    c.ak[e] = 8192u * kbw + 2048u * (u32)(r >> 3) + 64u * (u32)(r & 7) + 16u * ((u32)(2 * e + h) ^ (u32)((r >> 2) & 3));
    c.av[e] = 8192u * kbw + 2048u * e + 64u * (u32)(4 * h + q4) + 16u * ((u32)(2 * blk + (p4 >> 1)) ^ (u32)((2 * e + h) & 3)) + 8u * (u32)(p4 & 1);
  }
#pragma unroll
  for (int d = 0; d < 4; ++d) c.o[d] = f32x16{};
  c.l = 0.f; c.bs[0] = c.bs[1] = 0.f; c.pend = false; c.al = 1.f; c.mx = 0.f;
  STAMP(3);
  BAR();
  {
    c.sA = f32x16{};
#pragma unroll
    for (int i = 0; i < 8; ++i) c.sA = MFMA(lds128(L0 + KR + c.ak[i & 1] + 512u * (i >> 1)), c.qf[i], c.sA);
    float mx = c.sA[0];
#pragma unroll
    for (int i = 1; i < 16; ++i) mx = fmaxf(mx, c.sA[i]);
    c.mc = swap_max(mx) * CSC;
  }
#pragma unroll 1
  for (int t = 0; t < NT - 2; t += 2) {
    tile_step<true>(c, c.sA, c.sB, L0, t);
    tile_step<true>(c, c.sB, c.sA, L0, t + 1);
  }
  tile_step<true>(c, c.sA, c.sB, L0, NT - 2);
  tile_step<false>(c, c.sB, c.sA, L0, NT - 1);
  const float l = swap_add(c.l), mc = c.mc;
  STAMP(4);
  auto merge = [&](auto kbc) {
    constexpr int KB = decltype(kbc)::value;
    const float inv = 1.0f / l;
    const u32 reg = L0 + MS + 8192u * w, rin = L0 + MS + 8192u * (w ^ 2);
#pragma unroll
    for (int j = 0; j < 4; ++j) {
      const f32x16& t = c.o[2 * (1 - KB) + (j >> 1)];
      u32x4 f;
#pragma unroll
      for (int e = 0; e < 4; ++e) f[e] = cvtpk(t[8 * (j & 1) + 2 * e] * inv, t[8 * (j & 1) + 2 * e + 1] * inv);
      lds_st128u(reg + 1024u * j + 16u * lane, f);
    }
    if (lane < 32) *(LAS f32x2*)(size_t)(reg + 4096u + 8u * lane) = f32x2{mc, l};
    if (w == 0 && lane == 0) *(volatile LAS u64*)(size_t)(L0 + MISC) = tag;
    BAR();
    const f32x2 st = *(const LAS f32x2*)(size_t)(rin + 4096u + 8u * r);
    u32x4 f[4];
#pragma unroll
    for (int j = 0; j < 4; ++j) f[j] = lds128u(rin + 1024u * j + 16u * lane);
    const float mn = fmaxf(mc, st[0]);
    const float a1 = __builtin_amdgcn_exp2f(mc - mn), a2 = __builtin_amdgcn_exp2f(st[0] - mn) * st[1];
    const float iv = 1.0f / (l * a1 + a2);
    const float f1 = a1 * iv, f2 = a2 * iv;
#pragma unroll
    for (int j = 0; j < 4; ++j) {
      const f32x16& t = c.o[2 * KB + (j >> 1)];
      u32x4 o4;
#pragma unroll
      for (int e = 0; e < 4; ++e)
        o4[e] = cvtpk(t[8 * (j & 1) + 2 * e] * f1 + __uint_as_float(f[j][e] << 16) * f2, t[8 * (j & 1) + 2 * e + 1] * f1 + __uint_as_float(f[j][e] & 0xffff0000u) * f2);
      lds_st128u(rin + 1024u * j + 16u * lane, o4);
    }
  };
  if (kbw) merge(std::integral_constant<int, 1>{}); else merge(std::integral_constant<int, 0>{});
  STAMP(5);
  BAR();
  STAMP(6);
  {
    f32x16 acc[2] = {f32x16{}, f32x16{}};
    const u32 ao0 = L0 + 512u * (4u * h + q4) + 16u * ((8u * w + 2u * blk + (p4 >> 1)) ^ (4u * q4)) + 8u * (p4 & 1);
#pragma unroll
    for (int cc = 0; cc < 4; ++cc) {
      bf16x8 B[4], A[8];
      const u32 buf = cc == 0 ? KR : cc == 1 ? VR : cc == 2 ? KR + 2 * TB : EX;
#pragma unroll
      for (int kk = 0; kk < 4; ++kk) {
        B[kk] = lds128(ofrag(4u * ((u32)(cc + b) & 3u) + kk));
#pragma unroll
        for (int T = 0; T < 2; ++T) { const u32 a = buf + (ao0 ^ (64u * T)) + 8192u * kk; A[2 * kk + T] = trfrag(a, a + 4096u); }
      }
#pragma unroll
      for (int kk = 0; kk < 4; ++kk) { acc[0] = MFMA(A[2 * kk], B[kk], acc[0]); acc[1] = MFMA(A[2 * kk + 1], B[kk], acc[1]); }
    }
#pragma unroll
    for (int n = 0; n < 2; ++n)
#pragma unroll
      for (int u = 0; u < 2; ++u)
        __builtin_amdgcn_raw_buffer_store_b128(__builtin_bit_cast(u32x4, pack8(acc[n], u)), sW, 1024u * u + 16u * lane + WS_SL + (u32)((g * 16 + 4 * w + 2 + n) * 16 + si) * 2048u, 0, AUX_SC1);
    DRAIN();
    if (lane == 0) __hip_atomic_store((GAS u64*)(ws + WS_F3 + (g * 8 + wave) * 128) + si, tag, RLX_AGENT);
  }
  STAMP(7);
  poll_flags((const GAS u64*)(ws + WS_F3 + (g * 8 + (si >> 2) + ((si & 2) ? 0 : 4)) * 128), 15, lane, tag);
  f32x4 sum = {0.f, 0.f, 0.f, 0.f};
  {
    u32x2 v[16];
#pragma unroll
    for (int i = 0; i < 16; ++i)
      v[i] = __builtin_amdgcn_raw_buffer_load_b64(sW, 1024u * (w >> 1) + 16u * lane + 8u * (w & 1) + 2048u * i, WS_SL + (u32)((g * 16 + si) * 16) * 2048u, AUX_SC1);
#pragma unroll
    for (int i = 0; i < 16; ++i) {
      sum[0] += __uint_as_float(v[i][0] << 16); sum[1] += __uint_as_float(v[i][0] & 0xffff0000u);
      sum[2] += __uint_as_float(v[i][1] << 16); sum[3] += __uint_as_float(v[i][1] & 0xffff0000u);
    }
  }
  u32x2 ov = {cvtpk(sum[0], sum[1]), cvtpk(sum[2], sum[3])};
  *(GAS u32x2*)((char*)P.out + ((size_t)(b * 32 + r) * 1024 + pi * 512 + 32 * si + 8 * w + 4 * h) * 2) = ov;
  if (w == 0 && lane == 0) __hip_atomic_store((GAS u64*)(ws + WS_EP + g * 256), tag, RLX_AGENT);
  STAMP(8);
}

extern "C" void kernel_launch(void* const* d_in, const int* in_sizes, int n_in, void* d_out, int out_size, void* d_ws, size_t ws_size, hipStream_t stream) {
  (void)hipFuncSetAttribute((const void*)gqa_block_fused, hipFuncAttributeMaxDynamicSharedMemorySize, (int)LDS_BYTES);
  for (int i = 0; i < n_in; ++i) fprintf(stderr, "in[%d]=%p n=%d\n", i, d_in[i], in_sizes[i]);
  fprintf(stderr, "out=%p ws=%p wsz=%zu\n", d_out, d_ws, ws_size);
  Params P;
  for (int p = 0; p < 2; ++p) { P.x[p] = d_in[5 * p + 0]; P.k[p] = d_in[5 * p + 1]; P.v[p] = d_in[5 * p + 2]; P.wq[p] = d_in[5 * p + 3]; P.wo[p] = d_in[5 * p + 4]; }
  P.out = d_out; P.ws = d_ws;
  hipLaunchKernelGGL(gqa_block_fused, dim3(256), dim3(512), LDS_BYTES, stream, P);
}
```

```cpp
#include <hip/hip_runtime.h>
#include <stdint.h>
#include <type_traits>

typedef unsigned u32;
typedef unsigned long long u64;
typedef u32 u32x4 __attribute__((ext_vector_type(4)));
typedef u32 u32x2 __attribute__((ext_vector_type(2)));
typedef float f32x2 __attribute__((ext_vector_type(2)));
typedef float f32x4 __attribute__((ext_vector_type(4)));
typedef float f32x16 __attribute__((ext_vector_type(16)));
typedef __bf16 bf16x2 __attribute__((ext_vector_type(2)));
typedef __bf16 bf16x4 __attribute__((ext_vector_type(4)));
typedef __bf16 bf16x8 __attribute__((ext_vector_type(8)));
#define LAS __attribute__((address_space(3)))
#define GAS __attribute__((address_space(1)))
#define DI __device__ __forceinline__
#define RLX_AGENT __ATOMIC_RELAXED, __HIP_MEMORY_SCOPE_AGENT
#define SBAR() __builtin_amdgcn_sched_barrier(0)
#define PIN(x) asm volatile("" : "+v"(x))

constexpr u32 KR = 0, VR = 65536, TB = 16384, EX = 131072, LDS_BYTES = 163840;
constexpr u32 MS = 98304;
constexpr u32 MISC = MS + 6144;
constexpr int NT = 32, NREG = 6;
constexpr u32 WS_EP = 0;
constexpr u32 WS_F3 = 4096;
constexpr u32 WS_SL = 32768;
constexpr u32 WS_END = WS_SL + 16u * 16u * 16u * 2048u;
constexpr u32 WS_DBG = WS_END;
constexpr u64 TAGC = 0x9E3779B97F4A7C15ull;
constexpr float CSC = 0.088388347648318447f * 1.4426950408889634f;
constexpr float THR = 8.0f;
constexpr int AUX_SC1 = 16, AUX_NT = 2;
constexpr u32 SPIN_CAP = 1u << 18;

struct Params {
  const void* x[2]; const void* k[2]; const void* v[2]; const void* wq[2]; const void* wo[2];
  void* out; void* ws;
};

#define BAR() asm volatile("s_waitcnt lgkmcnt(0)\n\ts_barrier" ::: "memory")
#define DRAIN() asm volatile("s_waitcnt vmcnt(0)" ::: "memory")
#define LGKM0() asm volatile("s_waitcnt lgkmcnt(0)" ::: "memory")
template <int N> DI void waitvm() { asm volatile("s_waitcnt vmcnt(%0)" ::"i"(N) : "memory"); }

DI void dma(u32 m0v, u32 voff, __amdgpu_buffer_rsrc_t srd, u32 soff) {
  asm volatile("s_mov_b32 m0, %0\n\ts_nop 0\n\tbuffer_load_dwordx4 %1, %2, %3 offen nt lds" ::"s"(m0v), "v"(voff), "s"(srd), "s"(soff) : "memory");
}
DI void dma_t(u32 m0v, u32 voff, __amdgpu_buffer_rsrc_t srd, u32 soff) {
  asm volatile("s_mov_b32 m0, %0\n\ts_nop 0\n\tbuffer_load_dwordx4 %1, %2, %3 offen lds" ::"s"(m0v), "v"(voff), "s"(srd), "s"(soff) : "memory");
}
#define DMAW dma_t
DI u32 cvtpk(float lo, float hi) { f32x2 v = {lo, hi}; bf16x2 b = __builtin_convertvector(v, bf16x2); return __builtin_bit_cast(u32, b); }
DI bf16x8 pack8(const f32x16& x, int s) {
  u32x4 w = {cvtpk(x[8 * s], x[8 * s + 1]), cvtpk(x[8 * s + 2], x[8 * s + 3]), cvtpk(x[8 * s + 4], x[8 * s + 5]), cvtpk(x[8 * s + 6], x[8 * s + 7])};
  return __builtin_bit_cast(bf16x8, w);
}
DI bf16x8 lds128(u32 a) { return *(const LAS bf16x8*)(size_t)a; }
DI u32x4 lds128u(u32 a) { return *(const LAS u32x4*)(size_t)a; }
DI void lds_st128(u32 a, bf16x8 v) { *(LAS bf16x8*)(size_t)a = v; }
DI void lds_st128u(u32 a, u32x4 v) { *(LAS u32x4*)(size_t)a = v; }
DI bf16x8 trfrag(u32 a0, u32 a1) {
  bf16x4 lo = __builtin_amdgcn_ds_read_tr16_b64_v4bf16((LAS bf16x4*)(size_t)a0);
  bf16x4 hi = __builtin_amdgcn_ds_read_tr16_b64_v4bf16((LAS bf16x4*)(size_t)a1);
  return __builtin_shufflevector(lo, hi, 0, 1, 2, 3, 4, 5, 6, 7);
}
#define MFMA(a, b, c) __builtin_amdgcn_mfma_f32_32x32x16_bf16((a), (b), (c), 0, 0, 0)
DI float swap_max(float v) { auto r = __builtin_amdgcn_permlane32_swap(__float_as_uint(v), __float_as_uint(v), false, false); return fmaxf(__uint_as_float(r[0]), __uint_as_float(r[1])); }
DI float swap_add(float v) { auto r = __builtin_amdgcn_permlane32_swap(__float_as_uint(v), __float_as_uint(v), false, false); return __uint_as_float(r[0]) + __uint_as_float(r[1]); }
DI float max3(float a, float b, float c) { float r; asm("v_max3_f32 %0, %1, %2, %3" : "=v"(r) : "v"(a), "v"(b), "v"(c)); return r; }

DI void poll_flags(const GAS u64* base, int mask, int lane, u64 tag) {
  const GAS u64* p = base + (lane & mask);
#pragma unroll 1
  for (u32 spins = 0; spins < SPIN_CAP; ++spins) {
    u64 v = __hip_atomic_load(p, RLX_AGENT);
    if (__all(v == tag)) break;
    __builtin_amdgcn_s_sleep(1);
  }
  asm volatile("" ::: "memory");
}
#define STAMP(i) do {} while (0)

struct St {
  bf16x8 qf[8], kf[8], vf[8];
  f32x16 o[4], sA, sB;
  u32x4 p[2];
  float e[2][2];
  float bs[2];
  float l, mc, mx, al;
  bool pend;
  u32 ak[2], av[2];
};
template <int G> DI void sm_slice(St& c, const f32x16& s) {
  c.e[(G >> 1) & 1][G & 1] = __builtin_amdgcn_exp2f(fmaf(s[G], CSC, -c.mc));
  if constexpr (G >= 1) { constexpr int H = G - 1; c.bs[H & 1] += c.e[(H >> 1) & 1][H & 1]; PIN(c.bs[H & 1]); }
  if constexpr (G >= 2 && (G & 1) == 0) { constexpr int Q = (G - 2) >> 1; u32 w = cvtpk(c.e[Q & 1][0], c.e[Q & 1][1]); PIN(w); c.p[Q >> 2][Q & 3] = w; }
}
DI void sm_tail(St& c) {
  c.bs[1] += c.e[1][1];
  c.p[1][3] = cvtpk(c.e[1][0], c.e[1][1]);
  c.l += c.bs[0] + c.bs[1];
  c.bs[0] = 0.f; c.bs[1] = 0.f;
}
template <int I> DI bf16x8 ldk1(u32 a0, u32 a1) { return lds128(((I & 1) ? a1 : a0) + 512u * (I >> 1)); }
template <int I> DI bf16x8 ldv1(u32 a0, u32 a1) { return trfrag(a0 + 4096u * (I >> 2) + 512u * (I & 3), a1 + 4096u * (I >> 2) + 512u * (I & 3)); }
template <int I, bool QK> DI void qk_gap(St& c, f32x16& snew, const f32x16& scur, u32 v0, u32 v1) {
  if constexpr (QK) { if constexpr (I == 0) snew = MFMA(c.kf[0], c.qf[0], f32x16{}); else snew = MFMA(c.kf[I], c.qf[I], snew); PIN(snew); }
  c.vf[I] = ldv1<I>(v0, v1);
  if constexpr (I < 4) { sm_slice<4 + 2 * I>(c, scur); sm_slice<5 + 2 * I>(c, scur); }
  else sm_slice<8 + I>(c, scur);
  if constexpr (I == 7) sm_tail(c);
  SBAR();
}
template <int I, bool MX> DI void pv_gap(St& c, const f32x16& s) {
  c.o[I & 3] = MFMA(c.vf[I], __builtin_bit_cast(bf16x8, c.p[I >> 2]), c.o[I & 3]); PIN(c.o[I & 3]);
  if constexpr (MX) {
    if constexpr (I == 2) {
      const float m0 = fmaxf(s[0], s[1]);
      float ma = max3(m0, s[2], s[3]), mb = max3(m0, s[4], s[5]);
      ma = max3(ma, s[6], s[7]); mb = max3(mb, s[8], s[9]);
      c.mx = ma; c.al = mb;
    } else if constexpr (I == 3) {
      float ma = max3(c.mx, s[10], s[11]), mb = max3(c.al, s[12], s[13]);
      ma = max3(ma, s[14], s[15]);
      c.mx = fmaxf(ma, mb);
    } else if constexpr (I == 4) {
      c.pend = false;
      if (__builtin_expect(__any(fmaf(c.mx, CSC, -c.mc) > THR), 0)) {
        const float mn = fmaxf(c.mc, swap_max(c.mx) * CSC);
        c.al = __builtin_amdgcn_exp2f(c.mc - mn);
        c.l *= c.al; c.mc = mn; c.pend = true;
      }
    } else if constexpr (I == 7) {
      if (__builtin_expect(c.pend, 0)) {
#pragma unroll
        for (int d = 0; d < 4; ++d)
#pragma unroll
          for (int i = 0; i < 16; ++i) c.o[d][i] *= c.al;
      }
    }
  }
  SBAR();
}
#define G8(F, ...) F<0 __VA_ARGS__; F<1 __VA_ARGS__; F<2 __VA_ARGS__; F<3 __VA_ARGS__; F<4 __VA_ARGS__; F<5 __VA_ARGS__; F<6 __VA_ARGS__; F<7 __VA_ARGS__;
template <bool NEXT> DI void tile_step(St& c, f32x16& scur, f32x16& snew, u32 L0, int t) {
  u32 k0 = L0 + KR + (u32)((t + 1) & 3) * TB + c.ak[0], k1 = L0 + KR + (u32)((t + 1) & 3) * TB + c.ak[1];
  u32 v0 = L0 + VR + (u32)(t & 3) * TB + c.av[0], v1 = L0 + VR + (u32)(t & 3) * TB + c.av[1];
  asm("" : "+v"(k0)); asm("" : "+v"(k1)); asm("" : "+v"(v0)); asm("" : "+v"(v1));
  BAR();
  if constexpr (NEXT) {
    c.kf[0] = ldk1<0>(k0, k1); c.kf[1] = ldk1<1>(k0, k1); c.kf[2] = ldk1<2>(k0, k1); c.kf[3] = ldk1<3>(k0, k1);
    c.kf[4] = ldk1<4>(k0, k1); c.kf[5] = ldk1<5>(k0, k1); c.kf[6] = ldk1<6>(k0, k1); c.kf[7] = ldk1<7>(k0, k1);
  }
  SBAR();
  sm_slice<0>(c, scur); sm_slice<1>(c, scur); sm_slice<2>(c, scur); sm_slice<3>(c, scur);
  SBAR();
  G8(qk_gap, , NEXT>(c, snew, scur, v0, v1))
  if constexpr (!NEXT) BAR();
  G8(pv_gap, , NEXT>(c, snew))
}

extern "C" __global__ __launch_bounds__(512, 2) void gqa_block_fused(Params P) {
  extern __shared__ __attribute__((aligned(16))) char lds_[];
  const int tid = threadIdx.x, lane = tid & 63;
  const int wave = __builtin_amdgcn_readfirstlane(tid >> 6);
  const int bid = blockIdx.x;
  const int cidx = (bid & 7) * 4 + (bid >> 6), b = (bid >> 3) & 7;
  const int pi = cidx >> 4, kvh = (cidx >> 1) & 7, hh = cidx & 1;
  const int g = pi * 8 + b, si = kvh * 2 + hh;
  const u32 L0 = (u32)(size_t)lds_;
  char* ws = (char*)P.ws;
  const int h = lane >> 5, r = lane & 31, blk = (lane >> 4) & 1, q4 = (lane >> 2) & 3, p4 = lane & 3;
  const __amdgpu_buffer_rsrc_t sW = __builtin_amdgcn_make_buffer_rsrc(ws, 0, (int)WS_END, 0x00020000);
  auto ofrag = [&](u32 ks) -> u32 { const u32 dt = (ks >> 1) & 3u, wo_ = (ks >> 3) + 2u * (dt >> 1); return L0 + MS + 8192u * (wo_ ^ 2u) + 1024u * (2u * (dt & 1u) + (ks & 1u)) + 16u * lane; };
  STAMP(0);

  if (wave >= 4) {
    const int w = wave - 4;
    const size_t kvo = (size_t)(b * 8 + kvh) * 2048 * 256;
    const __amdgpu_buffer_rsrc_t sKV = __builtin_amdgcn_make_buffer_rsrc((char*)(w < 2 ? P.k[pi] : P.v[pi]) + kvo, 0, 524288, 0x00020000);
    const __amdgpu_buffer_rsrc_t sO = __builtin_amdgcn_make_buffer_rsrc((char*)P.wo[pi] + (size_t)(kvh * 4 + hh * 2) * 128 * 1024, 0, 262144, 0x00020000);
    u32 vk[2], vo[2];
#pragma unroll
    for (int i = 0; i < 2; ++i) {
      vk[i] = 256u * (u32)((lane >> 2) & 7) + 64u * (u32)h + 16u * ((u32)(lane & 3) ^ (u32)(2 * i + ((lane >> 4) & 1))) + 8192u * (u32)(w & 1);
      const u32 rm = (2 * i + h) & 3, ch = (u32)r ^ (rm << 2);
      vo[i] = (u32)h * 1024u + 256u * (ch >> 3) + 16u * (ch & 7u) + 128u;
    }
    const u32 vw = (u32)(lane >> 3) * 1024u + 16u * ((u32)(lane & 7) ^ (u32)(((lane >> 4) & 1) << 2)) + 256u * (u32)w;
    const u32 rb = L0 + (w < 2 ? KR : VR) + 8192u * (u32)(w & 1);
    u32x4 st[NREG][8];
    auto load_item = [&](int i) {
#pragma unroll
      for (int j = 0; j < 8; ++j) st[i][j] = __builtin_amdgcn_raw_buffer_load_b128(sKV, vk[(j >> 1) & 1] + 2048u * (j >> 1) + 128u * (j & 1), (u32)i * 16384u, AUX_NT);
      SBAR();
    };
    auto write_tile = [&](int i) {
#pragma unroll
      for (int j = 0; j < 8; ++j) lds_st128u(rb + (u32)(i & 3) * TB + 1024u * j + 16u * lane, st[i][j]);
      SBAR();
    };
    auto dma_tile = [&](int u) {
#pragma unroll
      for (int j = 0; j < 8; ++j) dma(rb + (u32)(u & 3) * TB + 1024u * j, vk[(j >> 1) & 1], sKV, (u32)u * 16384u + 2048u * (j >> 1) + 128u * (j & 1));
    };
    bf16x8 wr[2][16];
    const u32 aw0 = L0 + EX + 8192u * w + 128u * (4u * h + q4) + 16u * ((2u * blk + (p4 >> 1)) ^ (u32)((q4 >> 1) << 2)) + 8u * (p4 & 1);
    auto issue_w = [&](int cc) {
      const u32 c = (u32)(cc + b) & 3u;
#pragma unroll
      for (int i = 0; i < 8; ++i) DMAW(L0 + EX + 8192u * w + 1024u * i, vw, sO, (64u * c + 8u * i) * 1024u);
    };
    auto read_w = [&](int cc) {
#pragma unroll
      for (int kk = 0; kk < 4; ++kk)
#pragma unroll
        for (int T = 0; T < 2; ++T) { const u32 a = (aw0 ^ (64u * T)) + 2048u * kk; wr[T][4 * cc + kk] = trfrag(a, a + 1024u); }
      LGKM0();
      SBAR();
    };
    auto issue_wol = [&](int cc, u32 dst) {
      const u32 c = (u32)(cc + b) & 3u;
#pragma unroll
      for (int i = 0; i < 8; ++i) { const u32 I = 8u * w + i; DMAW(L0 + dst + 1024u * I, vo[i & 1], sO, (64u * c + 2u * I) * 1024u); }
    };
    BAR();
    load_item(0); load_item(1); load_item(2);
    BAR(); BAR(); BAR();
    load_item(3); load_item(4); load_item(5);
    BAR();
    write_tile(0); write_tile(1); write_tile(2); write_tile(3);
    BAR();
    auto stream = [&](auto offc) {
      constexpr int OFF = decltype(offc)::value;
#pragma unroll
      for (int t = 0; t < NT; ++t) {
        if (t - 3 + OFF >= NREG && t - 3 + OFF < NT) waitvm<16>();
        BAR();
        const int u = t + OFF;
        if (u >= 4 && u < NREG) write_tile(u);
        else if (u >= NREG && u < NT) dma_tile(u);
        if (t == 14 || t == 18 || t == 22 || t == 26) read_w((t - 14) >> 2);
        if (t == 10 || t == 14 || t == 18 || t == 22) issue_w((t - 10) >> 2);
        if (t == 27) issue_wol(3, EX);
        if (t == 29) issue_wol(0, KR);
        if (t == 30) issue_wol(1, VR);
        if (t == 31) issue_wol(2, KR + 2 * TB);
      }
    };
    if (w < 2) stream(std::integral_constant<int, 4>{}); else stream(std::integral_constant<int, 3>{});
    BAR();
    BAR();
    STAMP(5);
    DRAIN(); BAR();
    STAMP(6);
    const u64 tag = *(volatile LAS u64*)(size_t)(L0 + MISC);
    f32x16 acc[2] = {f32x16{}, f32x16{}};
    {
      bf16x8 B[16];
#pragma unroll
      for (int j = 0; j < 16; ++j) B[j] = lds128(ofrag(4u * ((u32)((j >> 2) + b) & 3u) + (j & 3)));
#pragma unroll
      for (int j = 0; j < 16; ++j) { acc[0] = MFMA(wr[0][j], B[j], acc[0]); acc[1] = MFMA(wr[1][j], B[j], acc[1]); }
    }
#pragma unroll
    for (int n = 0; n < 2; ++n)
#pragma unroll
      for (int u = 0; u < 2; ++u)
        __builtin_amdgcn_raw_buffer_store_b128(__builtin_bit_cast(u32x4, pack8(acc[n], u)), sW, 1024u * u + 16u * lane + WS_SL + (u32)((g * 16 + 4 * w + n) * 16 + si) * 2048u, 0, AUX_SC1);
    DRAIN();
    if (lane == 0) __hip_atomic_store((GAS u64*)(ws + WS_F3 + (g * 8 + wave) * 128) + si, tag, RLX_AGENT);
    STAMP(7);
    return;
  }

  const int w = wave;
  St c;
  u64 tag;
  {
    const __amdgpu_buffer_rsrc_t sX = __builtin_amdgcn_make_buffer_rsrc((char*)P.x[pi] + (size_t)b * 32768, 0, 32768, 0x00020000);
    const __amdgpu_buffer_rsrc_t sQ = __builtin_amdgcn_make_buffer_rsrc((char*)P.wq[pi] + (size_t)(kvh * 512 + hh * 256) * 2, 0, 511 * 8192 + 512, 0x00020000);
    const u32 vq = (u32)(lane >> 3) * 8192u + 16u * ((u32)(lane & 7) ^ (u32)(((lane >> 4) & 1) << 2)) + 128u * (u32)w;
    auto issue_wq = [&](int cc) {
      const u32 ch = (u32)(cc + b) & 7u;
#pragma unroll
      for (int i = 0; i < 8; ++i) DMAW(L0 + 32768u * w + 8192u * (cc & 3) + 1024u * i, vq, sQ, (64u * ch + 8u * i) * 8192u);
    };
    {
      const GAS u64* ep = (const GAS u64*)(ws + WS_EP + g * 256);
      asm volatile("global_load_dwordx2 %0, %1, off sc1" : "=v"(tag) : "v"(ep) : "memory");
    }
#pragma unroll
    for (int i = 0; i < 8; ++i) { const u32 q = 8u * w + i; DMAW(L0 + EX + 1024u * q, (16u * lane) ^ (16u * (q & 15u)), sX, 1024u * q); }
    issue_wq(0); issue_wq(1); issue_wq(2); issue_wq(3);
    BAR();
    waitvm<32>(); BAR();
    asm volatile("" : "+v"(tag));
    tag += TAGC;
    STAMP(1);
    bf16x8 xf[32];
#pragma unroll
    for (int j = 0; j < 32; ++j) {
      const u32 ks = 4u * ((u32)((j >> 2) + b) & 7u) + (j & 3);
      xf[j] = lds128(L0 + EX + 1024u * r + ((32u * ks + 16u * h) ^ (16u * (r & 15))));
    }
    BAR();
    f32x16 qa[2] = {f32x16{}, f32x16{}};
    const u32 aq0 = L0 + 32768u * w + 128u * (8u * h + q4) + 16u * ((2u * blk + (p4 & 1)) ^ (u32)((q4 >> 1) << 2)) + 8u * (p4 >> 1);
#pragma unroll
    for (int cc = 0; cc < 8; ++cc) {
      if (cc <= 4) waitvm<24>(); else if (cc == 5) waitvm<16>(); else if (cc == 6) waitvm<8>(); else waitvm<0>();
      bf16x8 af[8];
#pragma unroll
      for (int kk = 0; kk < 4; ++kk)
#pragma unroll
        for (int T = 0; T < 2; ++T) { const u32 a = (aq0 ^ (64u * T)) + 8192u * (cc & 3) + 2048u * kk; af[2 * kk + T] = trfrag(a, a + 512u); }
      LGKM0();
      if (cc < 4) issue_wq(cc + 4);
      if (cc == 3) BAR();
      SBAR();
#pragma unroll
      for (int kk = 0; kk < 4; ++kk)
#pragma unroll
        for (int T = 0; T < 2; ++T) qa[T] = MFMA(af[2 * kk + T], xf[4 * cc + kk], qa[T]);
    }
    STAMP(2);
    const u32 fo = (u32)((w >> 1) * 8 + 4 * (w & 1)) * 1024u + 16u * lane;
#pragma unroll
    for (int T = 0; T < 2; ++T)
#pragma unroll
      for (int s = 0; s < 2; ++s) lds_st128(L0 + EX + fo + 1024u * (2 * T + s), pack8(qa[T], s));
  }
  const int hl = w & 1, kbw = w >> 1;
  BAR();
#pragma unroll
  for (int ks = 0; ks < 8; ++ks) c.qf[ks] = lds128(L0 + EX + (u32)(hl * 8 + ks) * 1024u + 16u * lane);
#pragma unroll
  for (int e = 0; e < 2; ++e) {
    c.ak[e] = 8192u * kbw + 2048u * (u32)(r >> 3) + 64u * (u32)(r & 7) + 16u * ((u32)(2 * e + h) ^ (u32)((r >> 2) & 3));
    c.av[e] = 8192u * kbw + 2048u * e + 64u * (u32)(4 * h + q4) + 16u * ((u32)(2 * blk + (p4 >> 1)) ^ (u32)((2 * e + h) & 3)) + 8u * (u32)(p4 & 1);
  }
#pragma unroll
  for (int d = 0; d < 4; ++d) c.o[d] = f32x16{};
  c.l = 0.f; c.bs[0] = c.bs[1] = 0.f; c.pend = false; c.al = 1.f; c.mx = 0.f;
  STAMP(3);
  BAR();
  {
    c.sA = f32x16{};
#pragma unroll
    for (int i = 0; i < 8; ++i) c.sA = MFMA(lds128(L0 + KR + c.ak[i & 1] + 512u * (i >> 1)), c.qf[i], c.sA);
    float mx = c.sA[0];
#pragma unroll
    for (int i = 1; i < 16; ++i) mx = fmaxf(mx, c.sA[i]);
    c.mc = swap_max(mx) * CSC;
  }
#pragma unroll 1
  for (int t = 0; t < NT - 2; t += 2) {
    tile_step<true>(c, c.sA, c.sB, L0, t);
    tile_step<true>(c, c.sB, c.sA, L0, t + 1);
  }
  tile_step<true>(c, c.sA, c.sB, L0, NT - 2);
  tile_step<false>(c, c.sB, c.sA, L0, NT - 1);
  const float l = swap_add(c.l), mc = c.mc;
  STAMP(4);
  auto merge = [&](auto kbc) {
    constexpr int KB = decltype(kbc)::value;
    const float inv = 1.0f / l;
    const u32 reg = L0 + MS + 8192u * w, rin = L0 + MS + 8192u * (w ^ 2);
#pragma unroll
    for (int j = 0; j < 4; ++j) {
      const f32x16& t = c.o[2 * (1 - KB) + (j >> 1)];
      u32x4 f;
#pragma unroll
      for (int e = 0; e < 4; ++e) f[e] = cvtpk(t[8 * (j & 1) + 2 * e] * inv, t[8 * (j & 1) + 2 * e + 1] * inv);
      lds_st128u(reg + 1024u * j + 16u * lane, f);
    }
    if (lane < 32) *(LAS f32x2*)(size_t)(reg + 4096u + 8u * lane) = f32x2{mc, l};
    if (w == 0 && lane == 0) *(volatile LAS u64*)(size_t)(L0 + MISC) = tag;
    BAR();
    const f32x2 st = *(const LAS f32x2*)(size_t)(rin + 4096u + 8u * r);
    u32x4 f[4];
#pragma unroll
    for (int j = 0; j < 4; ++j) f[j] = lds128u(rin + 1024u * j + 16u * lane);
    const float mn = fmaxf(mc, st[0]);
    const float a1 = __builtin_amdgcn_exp2f(mc - mn), a2 = __builtin_amdgcn_exp2f(st[0] - mn) * st[1];
    const float iv = 1.0f / (l * a1 + a2);
    const float f1 = a1 * iv, f2 = a2 * iv;
#pragma unroll
    for (int j = 0; j < 4; ++j) {
      const f32x16& t = c.o[2 * KB + (j >> 1)];
      u32x4 o4;
#pragma unroll
      for (int e = 0; e < 4; ++e)
        o4[e] = cvtpk(t[8 * (j & 1) + 2 * e] * f1 + __uint_as_float(f[j][e] << 16) * f2, t[8 * (j & 1) + 2 * e + 1] * f1 + __uint_as_float(f[j][e] & 0xffff0000u) * f2);
      lds_st128u(rin + 1024u * j + 16u * lane, o4);
    }
  };
  if (kbw) merge(std::integral_constant<int, 1>{}); else merge(std::integral_constant<int, 0>{});
  STAMP(5);
  BAR();
  STAMP(6);
  {
    f32x16 acc[2] = {f32x16{}, f32x16{}};
    const u32 ao0 = L0 + 512u * (4u * h + q4) + 16u * ((8u * w + 2u * blk + (p4 >> 1)) ^ (4u * q4)) + 8u * (p4 & 1);
#pragma unroll
    for (int cc = 0; cc < 4; ++cc) {
      bf16x8 B[4], A[8];
      const u32 buf = cc == 0 ? KR : cc == 1 ? VR : cc == 2 ? KR + 2 * TB : EX;
#pragma unroll
      for (int kk = 0; kk < 4; ++kk) {
        B[kk] = lds128(ofrag(4u * ((u32)(cc + b) & 3u) + kk));
#pragma unroll
        for (int T = 0; T < 2; ++T) { const u32 a = buf + (ao0 ^ (64u * T)) + 8192u * kk; A[2 * kk + T] = trfrag(a, a + 4096u); }
      }
#pragma unroll
      for (int kk = 0; kk < 4; ++kk) { acc[0] = MFMA(A[2 * kk], B[kk], acc[0]); acc[1] = MFMA(A[2 * kk + 1], B[kk], acc[1]); }
    }
#pragma unroll
    for (int n = 0; n < 2; ++n)
#pragma unroll
      for (int u = 0; u < 2; ++u)
        __builtin_amdgcn_raw_buffer_store_b128(__builtin_bit_cast(u32x4, pack8(acc[n], u)), sW, 1024u * u + 16u * lane + WS_SL + (u32)((g * 16 + 4 * w + 2 + n) * 16 + si) * 2048u, 0, AUX_SC1);
    DRAIN();
    if (lane == 0) __hip_atomic_store((GAS u64*)(ws + WS_F3 + (g * 8 + wave) * 128) + si, tag, RLX_AGENT);
  }
  STAMP(7);
  poll_flags((const GAS u64*)(ws + WS_F3 + (g * 8 + (si >> 2) + ((si & 2) ? 0 : 4)) * 128), 15, lane, tag);
  f32x4 sum = {0.f, 0.f, 0.f, 0.f};
  {
    u32x2 v[16];
#pragma unroll
    for (int i = 0; i < 16; ++i)
      v[i] = __builtin_amdgcn_raw_buffer_load_b64(sW, 1024u * (w >> 1) + 16u * lane + 8u * (w & 1) + 2048u * i, WS_SL + (u32)((g * 16 + si) * 16) * 2048u, AUX_SC1);
#pragma unroll
    for (int i = 0; i < 16; ++i) {
      sum[0] += __uint_as_float(v[i][0] << 16); sum[1] += __uint_as_float(v[i][0] & 0xffff0000u);
      sum[2] += __uint_as_float(v[i][1] << 16); sum[3] += __uint_as_float(v[i][1] & 0xffff0000u);
    }
  }
  u32x2 ov = {cvtpk(sum[0], sum[1]), cvtpk(sum[2], sum[3])};
  *(GAS u32x2*)((char*)P.out + ((size_t)(b * 32 + r) * 1024 + pi * 512 + 32 * si + 8 * w + 4 * h) * 2) = ov;
  if (w == 0 && lane == 0) __hip_atomic_store((GAS u64*)(ws + WS_EP + g * 256), tag, RLX_AGENT);
  STAMP(8);
}

extern "C" void kernel_launch(void* const* d_in, const int* in_sizes, int n_in, void* d_out, int out_size, void* d_ws, size_t ws_size, hipStream_t stream) {
  (void)hipFuncSetAttribute((const void*)gqa_block_fused, hipFuncAttributeMaxDynamicSharedMemorySize, (int)LDS_BYTES);
  Params P;
  for (int p = 0; p < 2; ++p) { P.x[p] = d_in[5 * p + 0]; P.k[p] = d_in[5 * p + 1]; P.v[p] = d_in[5 * p + 2]; P.wq[p] = d_in[5 * p + 3]; P.wo[p] = d_in[5 * p + 4]; }
  P.out = d_out; P.ws = d_ws;
  hipLaunchKernelGGL(gqa_block_fused, dim3(256), dim3(512), LDS_BYTES, stream, P);
}
```

```cpp
#include <hip/hip_runtime.h>
#include <stdint.h>
#include <stdio.h>
#include <type_traits>

typedef unsigned u32;
typedef unsigned long long u64;
typedef u32 u32x4 __attribute__((ext_vector_type(4)));
typedef u32 u32x2 __attribute__((ext_vector_type(2)));
typedef float f32x2 __attribute__((ext_vector_type(2)));
typedef float f32x4 __attribute__((ext_vector_type(4)));
typedef float f32x16 __attribute__((ext_vector_type(16)));
typedef __bf16 bf16x2 __attribute__((ext_vector_type(2)));
typedef __bf16 bf16x4 __attribute__((ext_vector_type(4)));
typedef __bf16 bf16x8 __attribute__((ext_vector_type(8)));
#define LAS __attribute__((address_space(3)))
#define GAS __attribute__((address_space(1)))
#define DI __device__ __forceinline__
#define RLX_AGENT __ATOMIC_RELAXED, __HIP_MEMORY_SCOPE_AGENT
#define SBAR() __builtin_amdgcn_sched_barrier(0)
#define PIN(x) asm volatile("" : "+v"(x))

constexpr u32 KR = 0, VR = 65536, TB = 16384, EX = 131072, LDS_BYTES = 163840;
constexpr u32 MS = 98304;
constexpr u32 MISC = MS + 6144;
constexpr int NT = 32, NREG = 6;
constexpr u32 WS_EP = 0;
constexpr u32 WS_F3 = 4096;
constexpr u32 WS_SL = 32768;
constexpr u32 WS_END = WS_SL + 16u * 16u * 16u * 2048u;
constexpr u32 WS_DBG = WS_END;
constexpr u64 TAGC = 0x9E3779B97F4A7C15ull;
constexpr float CSC = 0.088388347648318447f * 1.4426950408889634f;
constexpr float THR = 8.0f;
constexpr int AUX_SC1 = 16, AUX_NT = 0;
constexpr u32 SPIN_CAP = 1u << 18;

struct Params {
  const void* x[2]; const void* k[2]; const void* v[2]; const void* wq[2]; const void* wo[2];
  void* out; void* ws;
};

#define BAR() asm volatile("s_waitcnt lgkmcnt(0)\n\ts_barrier" ::: "memory")
#define DRAIN() asm volatile("s_waitcnt vmcnt(0)" ::: "memory")
#define LGKM0() asm volatile("s_waitcnt lgkmcnt(0)" ::: "memory")
template <int N> DI void waitvm() { asm volatile("s_waitcnt vmcnt(%0)" ::"i"(N) : "memory"); }

DI void dma(u32 m0v, u32 voff, __amdgpu_buffer_rsrc_t srd, u32 soff) {
  asm volatile("s_mov_b32 m0, %0\n\ts_nop 0\n\tbuffer_load_dwordx4 %1, %2, %3 offen lds" ::"s"(m0v), "v"(voff), "s"(srd), "s"(soff) : "memory");
}
DI u32 cvtpk(float lo, float hi) { f32x2 v = {lo, hi}; bf16x2 b = __builtin_convertvector(v, bf16x2); return __builtin_bit_cast(u32, b); }
DI bf16x8 pack8(const f32x16& x, int s) {
  u32x4 w = {cvtpk(x[8 * s], x[8 * s + 1]), cvtpk(x[8 * s + 2], x[8 * s + 3]), cvtpk(x[8 * s + 4], x[8 * s + 5]), cvtpk(x[8 * s + 6], x[8 * s + 7])};
  return __builtin_bit_cast(bf16x8, w);
}
DI bf16x8 lds128(u32 a) { return *(const LAS bf16x8*)(size_t)a; }
DI u32x4 lds128u(u32 a) { return *(const LAS u32x4*)(size_t)a; }
DI void lds_st128(u32 a, bf16x8 v) { *(LAS bf16x8*)(size_t)a = v; }
DI void lds_st128u(u32 a, u32x4 v) { *(LAS u32x4*)(size_t)a = v; }
DI bf16x8 trfrag(u32 a0, u32 a1) {
  bf16x4 lo = __builtin_amdgcn_ds_read_tr16_b64_v4bf16((LAS bf16x4*)(size_t)a0);
  bf16x4 hi = __builtin_amdgcn_ds_read_tr16_b64_v4bf16((LAS bf16x4*)(size_t)a1);
  return __builtin_shufflevector(lo, hi, 0, 1, 2, 3, 4, 5, 6, 7);
}
#define MFMA(a, b, c) __builtin_amdgcn_mfma_f32_32x32x16_bf16((a), (b), (c), 0, 0, 0)
DI float swap_max(float v) { auto r = __builtin_amdgcn_permlane32_swap(__float_as_uint(v), __float_as_uint(v), false, false); return fmaxf(__uint_as_float(r[0]), __uint_as_float(r[1])); }
DI float swap_add(float v) { auto r = __builtin_amdgcn_permlane32_swap(__float_as_uint(v), __float_as_uint(v), false, false); return __uint_as_float(r[0]) + __uint_as_float(r[1]); }
DI float max3(float a, float b, float c) { float r; asm("v_max3_f32 %0, %1, %2, %3" : "=v"(r) : "v"(a), "v"(b), "v"(c)); return r; }

DI void poll_flags(const GAS u64* base, int mask, int lane, u64 tag) {
  const GAS u64* p = base + (lane & mask);
#pragma unroll 1
  for (u32 spins = 0; spins < SPIN_CAP; ++spins) {
    u64 v = __hip_atomic_load(p, RLX_AGENT);
    if (__all(v == tag)) break;
    __builtin_amdgcn_s_sleep(1);
  }
  asm volatile("" ::: "memory");
}
#define STAMP(i) do { rs_[(i) & 15] = __builtin_amdgcn_s_memrealtime(); if ((i) == 3 || (i) == 4) cs_[(i) - 3] = __builtin_amdgcn_s_memtime(); } while (0)

struct St {
  bf16x8 qf[8], kf[8], vf[8];
  f32x16 o[4], sA, sB;
  u32x4 p[2];
  float e[2][2];
  float bs[2];
  float l, mc, mx, al;
  bool pend;
  u32 ak[2], av[2];
};
template <int G> DI void sm_slice(St& c, const f32x16& s) {
  c.e[(G >> 1) & 1][G & 1] = __builtin_amdgcn_exp2f(fmaf(s[G], CSC, -c.mc));
  if constexpr (G >= 1) { constexpr int H = G - 1; c.bs[H & 1] += c.e[(H >> 1) & 1][H & 1]; PIN(c.bs[H & 1]); }
  if constexpr (G >= 2 && (G & 1) == 0) { constexpr int Q = (G - 2) >> 1; u32 w = cvtpk(c.e[Q & 1][0], c.e[Q & 1][1]); PIN(w); c.p[Q >> 2][Q & 3] = w; }
}
DI void sm_tail(St& c) {
  c.bs[1] += c.e[1][1];
  c.p[1][3] = cvtpk(c.e[1][0], c.e[1][1]);
  c.l += c.bs[0] + c.bs[1];
  c.bs[0] = 0.f; c.bs[1] = 0.f;
}
template <int I> DI bf16x8 ldk1(u32 a0, u32 a1) { return lds128(((I & 1) ? a1 : a0) + 512u * (I >> 1)); }
template <int I> DI bf16x8 ldv1(u32 a0, u32 a1) { return trfrag(a0 + 4096u * (I >> 2) + 512u * (I & 3), a1 + 4096u * (I >> 2) + 512u * (I & 3)); }
template <int I, bool QK> DI void qk_gap(St& c, f32x16& snew, const f32x16& scur, u32 v0, u32 v1) {
  if constexpr (QK) { if constexpr (I == 0) snew = MFMA(c.kf[0], c.qf[0], f32x16{}); else snew = MFMA(c.kf[I], c.qf[I], snew); PIN(snew); }
  c.vf[I] = ldv1<I>(v0, v1);
  if constexpr (I < 4) { sm_slice<4 + 2 * I>(c, scur); sm_slice<5 + 2 * I>(c, scur); }
  else sm_slice<8 + I>(c, scur);
  if constexpr (I == 7) sm_tail(c);
  SBAR();
}
template <int I, bool MX> DI void pv_gap(St& c, const f32x16& s) {
  c.o[I & 3] = MFMA(c.vf[I], __builtin_bit_cast(bf16x8, c.p[I >> 2]), c.o[I & 3]); PIN(c.o[I & 3]);
  if constexpr (MX) {
    if constexpr (I == 2) {
      const float m0 = fmaxf(s[0], s[1]);
      float ma = max3(m0, s[2], s[3]), mb = max3(m0, s[4], s[5]);
      ma = max3(ma, s[6], s[7]); mb = max3(mb, s[8], s[9]);
      c.mx = ma; c.al = mb;
    } else if constexpr (I == 3) {
      float ma = max3(c.mx, s[10], s[11]), mb = max3(c.al, s[12], s[13]);
      ma = max3(ma, s[14], s[15]);
      c.mx = fmaxf(ma, mb);
    } else if constexpr (I == 4) {
      c.pend = false;
      if (__builtin_expect(__any(fmaf(c.mx, CSC, -c.mc) > THR), 0)) {
        const float mn = fmaxf(c.mc, swap_max(c.mx) * CSC);
        c.al = __builtin_amdgcn_exp2f(c.mc - mn);
        c.l *= c.al; c.mc = mn; c.pend = true;
      }
    } else if constexpr (I == 7) {
      if (__builtin_expect(c.pend, 0)) {
#pragma unroll
        for (int d = 0; d < 4; ++d)
#pragma unroll
          for (int i = 0; i < 16; ++i) c.o[d][i] *= c.al;
      }
    }
  }
  SBAR();
}
#define G8(F, ...) F<0 __VA_ARGS__; F<1 __VA_ARGS__; F<2 __VA_ARGS__; F<3 __VA_ARGS__; F<4 __VA_ARGS__; F<5 __VA_ARGS__; F<6 __VA_ARGS__; F<7 __VA_ARGS__;
template <bool NEXT> DI void tile_step(St& c, f32x16& scur, f32x16& snew, u32 L0, int t) {
  u32 k0 = L0 + KR + (u32)((t + 1) & 3) * TB + c.ak[0], k1 = L0 + KR + (u32)((t + 1) & 3) * TB + c.ak[1];
  u32 v0 = L0 + VR + (u32)(t & 3) * TB + c.av[0], v1 = L0 + VR + (u32)(t & 3) * TB + c.av[1];
  asm("" : "+v"(k0)); asm("" : "+v"(k1)); asm("" : "+v"(v0)); asm("" : "+v"(v1));
  BAR();
  if constexpr (NEXT) {
    c.kf[0] = ldk1<0>(k0, k1); c.kf[1] = ldk1<1>(k0, k1); c.kf[2] = ldk1<2>(k0, k1); c.kf[3] = ldk1<3>(k0, k1);
    c.kf[4] = ldk1<4>(k0, k1); c.kf[5] = ldk1<5>(k0, k1); c.kf[6] = ldk1<6>(k0, k1); c.kf[7] = ldk1<7>(k0, k1);
  }
  SBAR();
  sm_slice<0>(c, scur); sm_slice<1>(c, scur); sm_slice<2>(c, scur); sm_slice<3>(c, scur);
  SBAR();
  G8(qk_gap, , NEXT>(c, snew, scur, v0, v1))
  if constexpr (!NEXT) BAR();
  G8(pv_gap, , NEXT>(c, snew))
}

extern "C" __global__ __launch_bounds__(512, 2) void gqa_block_fused(Params P) {
  extern __shared__ __attribute__((aligned(16))) char lds_[];
  const int tid = threadIdx.x, lane = tid & 63;
  const int wave = __builtin_amdgcn_readfirstlane(tid >> 6);
  const int bid = blockIdx.x;
  const int cidx = (bid & 7) * 4 + (bid >> 6), b = (bid >> 3) & 7;
  const int pi = cidx >> 4, kvh = (cidx >> 1) & 7, hh = cidx & 1;
  const int g = pi * 8 + b, si = kvh * 2 + hh;
  const u32 L0 = (u32)(size_t)lds_;
  char* ws = (char*)P.ws;
  const int h = lane >> 5, r = lane & 31, blk = (lane >> 4) & 1, q4 = (lane >> 2) & 3, p4 = lane & 3;
  const __amdgpu_buffer_rsrc_t sW = __builtin_amdgcn_make_buffer_rsrc(ws, 0, (int)WS_END, 0x00020000);
  auto ofrag = [&](u32 ks) -> u32 { const u32 dt = (ks >> 1) & 3u, wo_ = (ks >> 3) + 2u * (dt >> 1); return L0 + MS + 8192u * (wo_ ^ 2u) + 1024u * (2u * (dt & 1u) + (ks & 1u)) + 16u * lane; };
  u64 rs_[16]; u64 cs_[2];
  STAMP(0);

  if (wave >= 4) {
    const int w = wave - 4;
    const size_t kvo = (size_t)(b * 8 + kvh) * 2048 * 256;
    const __amdgpu_buffer_rsrc_t sKV = __builtin_amdgcn_make_buffer_rsrc((char*)(w < 2 ? P.k[pi] : P.v[pi]) + kvo, 0, 524288, 0x00020000);
    const __amdgpu_buffer_rsrc_t sO = __builtin_amdgcn_make_buffer_rsrc((char*)P.wo[pi] + (size_t)(kvh * 4 + hh * 2) * 128 * 1024, 0, 262144, 0x00020000);
    u32 vk[2], vo[2];
#pragma unroll
    for (int i = 0; i < 2; ++i) {
      vk[i] = 256u * (u32)((lane >> 2) & 7) + 64u * (u32)h + 16u * ((u32)(lane & 3) ^ (u32)(2 * i + ((lane >> 4) & 1))) + 8192u * (u32)(w & 1);
      const u32 rm = (2 * i + h) & 3, ch = (u32)r ^ (rm << 2);
      vo[i] = (u32)h * 1024u + 256u * (ch >> 3) + 16u * (ch & 7u) + 128u;
    }
    const u32 vw = (u32)(lane >> 3) * 1024u + 16u * ((u32)(lane & 7) ^ (u32)(((lane >> 4) & 1) << 2)) + 256u * (u32)w;
    const u32 rb = L0 + (w < 2 ? KR : VR) + 8192u * (u32)(w & 1);
    u32x4 st[NREG][8];
    auto load_item = [&](int i) {
#pragma unroll
      for (int j = 0; j < 8; ++j) st[i][j] = __builtin_amdgcn_raw_buffer_load_b128(sKV, vk[(j >> 1) & 1] + 2048u * (j >> 1) + 128u * (j & 1), (u32)i * 16384u, AUX_NT);
      SBAR();
    };
    auto write_tile = [&](int i) {
#pragma unroll
      for (int j = 0; j < 8; ++j) lds_st128u(rb + (u32)(i & 3) * TB + 1024u * j + 16u * lane, st[i][j]);
      SBAR();
    };
    auto dma_tile = [&](int u) {
#pragma unroll
      for (int j = 0; j < 8; ++j) dma(rb + (u32)(u & 3) * TB + 1024u * j, vk[(j >> 1) & 1], sKV, (u32)u * 16384u + 2048u * (j >> 1) + 128u * (j & 1));
    };
    bf16x8 wr[2][16];
    const u32 aw0 = L0 + EX + 8192u * w + 128u * (4u * h + q4) + 16u * ((2u * blk + (p4 >> 1)) ^ (u32)((q4 >> 1) << 2)) + 8u * (p4 & 1);
    auto issue_w = [&](int cc) {
      const u32 c = (u32)(cc + b) & 3u;
#pragma unroll
      for (int i = 0; i < 8; ++i) dma(L0 + EX + 8192u * w + 1024u * i, vw, sO, (64u * c + 8u * i) * 1024u);
    };
    auto read_w = [&](int cc) {
#pragma unroll
      for (int kk = 0; kk < 4; ++kk)
#pragma unroll
        for (int T = 0; T < 2; ++T) { const u32 a = (aw0 ^ (64u * T)) + 2048u * kk; wr[T][4 * cc + kk] = trfrag(a, a + 1024u); }
      LGKM0();
      SBAR();
    };
    auto issue_wol = [&](int cc, u32 dst) {
      const u32 c = (u32)(cc + b) & 3u;
#pragma unroll
      for (int i = 0; i < 8; ++i) { const u32 I = 8u * w + i; dma(L0 + dst + 1024u * I, vo[i & 1], sO, (64u * c + 2u * I) * 1024u); }
    };
    BAR();
    load_item(0); load_item(1); load_item(2);
    BAR(); BAR(); BAR();
    load_item(3); load_item(4); load_item(5);
    BAR();
    write_tile(0); write_tile(1); write_tile(2); write_tile(3);
    BAR();
    auto stream = [&](auto offc) {
      constexpr int OFF = decltype(offc)::value;
#pragma unroll
      for (int t = 0; t < NT; ++t) {
        if (t - 3 + OFF >= NREG && t - 3 + OFF < NT) waitvm<16>();
        BAR();
        const int u = t + OFF;
        if (u >= 4 && u < NREG) write_tile(u);
        else if (u >= NREG && u < NT) dma_tile(u);
        if (t == 14 || t == 18 || t == 22 || t == 26) read_w((t - 14) >> 2);
        if (t == 10 || t == 14 || t == 18 || t == 22) issue_w((t - 10) >> 2);
        if (t == 27) issue_wol(3, EX);
        if (t == 29) issue_wol(0, KR);
        if (t == 30) issue_wol(1, VR);
        if (t == 31) issue_wol(2, KR + 2 * TB);
      }
    };
    if (w < 2) stream(std::integral_constant<int, 4>{}); else stream(std::integral_constant<int, 3>{});
    BAR();
    BAR();
    STAMP(5);
    DRAIN(); BAR();
    STAMP(6);
    const u64 tag = *(volatile LAS u64*)(size_t)(L0 + MISC);
    f32x16 acc[2] = {f32x16{}, f32x16{}};
    {
      bf16x8 B[16];
#pragma unroll
      for (int j = 0; j < 16; ++j) B[j] = lds128(ofrag(4u * ((u32)((j >> 2) + b) & 3u) + (j & 3)));
#pragma unroll
      for (int j = 0; j < 16; ++j) { acc[0] = MFMA(wr[0][j], B[j], acc[0]); acc[1] = MFMA(wr[1][j], B[j], acc[1]); }
    }
#pragma unroll
    for (int n = 0; n < 2; ++n)
#pragma unroll
      for (int u = 0; u < 2; ++u)
        __builtin_amdgcn_raw_buffer_store_b128(__builtin_bit_cast(u32x4, pack8(acc[n], u)), sW, 1024u * u + 16u * lane + WS_SL + (u32)((g * 16 + 4 * w + n) * 16 + si) * 2048u, 0, AUX_SC1);
    DRAIN();
    if (lane == 0) __hip_atomic_store((GAS u64*)(ws + WS_F3 + (g * 8 + wave) * 128) + si, tag, RLX_AGENT);
    STAMP(7);
    return;
  }

  const int w = wave;
  St c;
  u64 tag;
  {
    const __amdgpu_buffer_rsrc_t sX = __builtin_amdgcn_make_buffer_rsrc((char*)P.x[pi] + (size_t)b * 32768, 0, 32768, 0x00020000);
    const __amdgpu_buffer_rsrc_t sQ = __builtin_amdgcn_make_buffer_rsrc((char*)P.wq[pi] + (size_t)(kvh * 512 + hh * 256) * 2, 0, 511 * 8192 + 512, 0x00020000);
    const u32 vq = (u32)(lane >> 3) * 8192u + 16u * ((u32)(lane & 7) ^ (u32)(((lane >> 4) & 1) << 2)) + 128u * (u32)w;
    auto issue_wq = [&](int cc) {
      const u32 ch = (u32)(cc + b) & 7u;
#pragma unroll
      for (int i = 0; i < 8; ++i) dma(L0 + 32768u * w + 8192u * (cc & 3) + 1024u * i, vq, sQ, (64u * ch + 8u * i) * 8192u);
    };
    {
      const GAS u64* ep = (const GAS u64*)(ws + WS_EP + g * 256);
      asm volatile("global_load_dwordx2 %0, %1, off sc1" : "=v"(tag) : "v"(ep) : "memory");
    }
#pragma unroll
    for (int i = 0; i < 8; ++i) { const u32 q = 8u * w + i; dma(L0 + EX + 1024u * q, (16u * lane) ^ (16u * (q & 15u)), sX, 1024u * q); }
    issue_wq(0); issue_wq(1); issue_wq(2); issue_wq(3);
    BAR();
    waitvm<32>(); BAR();
    asm volatile("" : "+v"(tag));
    tag += TAGC;
    STAMP(1);
    bf16x8 xf[32];
#pragma unroll
    for (int j = 0; j < 32; ++j) {
      const u32 ks = 4u * ((u32)((j >> 2) + b) & 7u) + (j & 3);
      xf[j] = lds128(L0 + EX + 1024u * r + ((32u * ks + 16u * h) ^ (16u * (r & 15))));
    }
    BAR();
    f32x16 qa[2] = {f32x16{}, f32x16{}};
    const u32 aq0 = L0 + 32768u * w + 128u * (8u * h + q4) + 16u * ((2u * blk + (p4 & 1)) ^ (u32)((q4 >> 1) << 2)) + 8u * (p4 >> 1);
#pragma unroll
    for (int cc = 0; cc < 8; ++cc) {
      if (cc <= 4) waitvm<24>(); else if (cc == 5) waitvm<16>(); else if (cc == 6) waitvm<8>(); else waitvm<0>();
      bf16x8 af[8];
#pragma unroll
      for (int kk = 0; kk < 4; ++kk)
#pragma unroll
        for (int T = 0; T < 2; ++T) { const u32 a = (aq0 ^ (64u * T)) + 8192u * (cc & 3) + 2048u * kk; af[2 * kk + T] = trfrag(a, a + 512u); }
      LGKM0();
      if (cc < 4) issue_wq(cc + 4);
      if (cc == 3) BAR();
      SBAR();
#pragma unroll
      for (int kk = 0; kk < 4; ++kk)
#pragma unroll
        for (int T = 0; T < 2; ++T) qa[T] = MFMA(af[2 * kk + T], xf[4 * cc + kk], qa[T]);
    }
    STAMP(2);
    const u32 fo = (u32)((w >> 1) * 8 + 4 * (w & 1)) * 1024u + 16u * lane;
#pragma unroll
    for (int T = 0; T < 2; ++T)
#pragma unroll
      for (int s = 0; s < 2; ++s) lds_st128(L0 + EX + fo + 1024u * (2 * T + s), pack8(qa[T], s));
  }
  const int hl = w & 1, kbw = w >> 1;
  BAR();
#pragma unroll
  for (int ks = 0; ks < 8; ++ks) c.qf[ks] = lds128(L0 + EX + (u32)(hl * 8 + ks) * 1024u + 16u * lane);
#pragma unroll
  for (int e = 0; e < 2; ++e) {
    c.ak[e] = 8192u * kbw + 2048u * (u32)(r >> 3) + 64u * (u32)(r & 7) + 16u * ((u32)(2 * e + h) ^ (u32)((r >> 2) & 3));
    c.av[e] = 8192u * kbw + 2048u * e + 64u * (u32)(4 * h + q4) + 16u * ((u32)(2 * blk + (p4 >> 1)) ^ (u32)((2 * e + h) & 3)) + 8u * (u32)(p4 & 1);
  }
#pragma unroll
  for (int d = 0; d < 4; ++d) c.o[d] = f32x16{};
  c.l = 0.f; c.bs[0] = c.bs[1] = 0.f; c.pend = false; c.al = 1.f; c.mx = 0.f;
  STAMP(3);
  BAR();
  {
    c.sA = f32x16{};
#pragma unroll
    for (int i = 0; i < 8; ++i) c.sA = MFMA(lds128(L0 + KR + c.ak[i & 1] + 512u * (i >> 1)), c.qf[i], c.sA);
    float mx = c.sA[0];
#pragma unroll
    for (int i = 1; i < 16; ++i) mx = fmaxf(mx, c.sA[i]);
    c.mc = swap_max(mx) * CSC;
  }
#pragma unroll 1
  for (int t = 0; t < NT - 2; t += 2) {
    tile_step<true>(c, c.sA, c.sB, L0, t);
    tile_step<true>(c, c.sB, c.sA, L0, t + 1);
  }
  tile_step<true>(c, c.sA, c.sB, L0, NT - 2);
  tile_step<false>(c, c.sB, c.sA, L0, NT - 1);
  const float l = swap_add(c.l), mc = c.mc;
  STAMP(4);
  auto merge = [&](auto kbc) {
    constexpr int KB = decltype(kbc)::value;
    const float inv = 1.0f / l;
    const u32 reg = L0 + MS + 8192u * w, rin = L0 + MS + 8192u * (w ^ 2);
#pragma unroll
    for (int j = 0; j < 4; ++j) {
      const f32x16& t = c.o[2 * (1 - KB) + (j >> 1)];
      u32x4 f;
#pragma unroll
      for (int e = 0; e < 4; ++e) f[e] = cvtpk(t[8 * (j & 1) + 2 * e] * inv, t[8 * (j & 1) + 2 * e + 1] * inv);
      lds_st128u(reg + 1024u * j + 16u * lane, f);
    }
    if (lane < 32) *(LAS f32x2*)(size_t)(reg + 4096u + 8u * lane) = f32x2{mc, l};
    if (w == 0 && lane == 0) *(volatile LAS u64*)(size_t)(L0 + MISC) = tag;
    BAR();
    const f32x2 st = *(const LAS f32x2*)(size_t)(rin + 4096u + 8u * r);
    u32x4 f[4];
#pragma unroll
    for (int j = 0; j < 4; ++j) f[j] = lds128u(rin + 1024u * j + 16u * lane);
    const float mn = fmaxf(mc, st[0]);
    const float a1 = __builtin_amdgcn_exp2f(mc - mn), a2 = __builtin_amdgcn_exp2f(st[0] - mn) * st[1];
    const float iv = 1.0f / (l * a1 + a2);
    const float f1 = a1 * iv, f2 = a2 * iv;
#pragma unroll
    for (int j = 0; j < 4; ++j) {
      const f32x16& t = c.o[2 * KB + (j >> 1)];
      u32x4 o4;
#pragma unroll
      for (int e = 0; e < 4; ++e)
        o4[e] = cvtpk(t[8 * (j & 1) + 2 * e] * f1 + __uint_as_float(f[j][e] << 16) * f2, t[8 * (j & 1) + 2 * e + 1] * f1 + __uint_as_float(f[j][e] & 0xffff0000u) * f2);
      lds_st128u(rin + 1024u * j + 16u * lane, o4);
    }
  };
  if (kbw) merge(std::integral_constant<int, 1>{}); else merge(std::integral_constant<int, 0>{});
  STAMP(5);
  BAR();
  STAMP(6);
  {
    f32x16 acc[2] = {f32x16{}, f32x16{}};
    const u32 ao0 = L0 + 512u * (4u * h + q4) + 16u * ((8u * w + 2u * blk + (p4 >> 1)) ^ (4u * q4)) + 8u * (p4 & 1);
#pragma unroll
    for (int cc = 0; cc < 4; ++cc) {
      bf16x8 B[4], A[8];
      const u32 buf = cc == 0 ? KR : cc == 1 ? VR : cc == 2 ? KR + 2 * TB : EX;
#pragma unroll
      for (int kk = 0; kk < 4; ++kk) {
        B[kk] = lds128(ofrag(4u * ((u32)(cc + b) & 3u) + kk));
#pragma unroll
        for (int T = 0; T < 2; ++T) { const u32 a = buf + (ao0 ^ (64u * T)) + 8192u * kk; A[2 * kk + T] = trfrag(a, a + 4096u); }
      }
#pragma unroll
      for (int kk = 0; kk < 4; ++kk) { acc[0] = MFMA(A[2 * kk], B[kk], acc[0]); acc[1] = MFMA(A[2 * kk + 1], B[kk], acc[1]); }
    }
#pragma unroll
    for (int n = 0; n < 2; ++n)
#pragma unroll
      for (int u = 0; u < 2; ++u)
        __builtin_amdgcn_raw_buffer_store_b128(__builtin_bit_cast(u32x4, pack8(acc[n], u)), sW, 1024u * u + 16u * lane + WS_SL + (u32)((g * 16 + 4 * w + 2 + n) * 16 + si) * 2048u, 0, AUX_SC1);
    DRAIN();
    if (lane == 0) __hip_atomic_store((GAS u64*)(ws + WS_F3 + (g * 8 + wave) * 128) + si, tag, RLX_AGENT);
  }
  STAMP(7);
  poll_flags((const GAS u64*)(ws + WS_F3 + (g * 8 + (si >> 2) + ((si & 2) ? 0 : 4)) * 128), 15, lane, tag);
  f32x4 sum = {0.f, 0.f, 0.f, 0.f};
  {
    u32x2 v[16];
#pragma unroll
    for (int i = 0; i < 16; ++i)
      v[i] = __builtin_amdgcn_raw_buffer_load_b64(sW, 1024u * (w >> 1) + 16u * lane + 8u * (w & 1) + 2048u * i, WS_SL + (u32)((g * 16 + si) * 16) * 2048u, AUX_SC1);
#pragma unroll
    for (int i = 0; i < 16; ++i) {
      sum[0] += __uint_as_float(v[i][0] << 16); sum[1] += __uint_as_float(v[i][0] & 0xffff0000u);
      sum[2] += __uint_as_float(v[i][1] << 16); sum[3] += __uint_as_float(v[i][1] & 0xffff0000u);
    }
  }
  u32x2 ov = {cvtpk(sum[0], sum[1]), cvtpk(sum[2], sum[3])};
  *(GAS u32x2*)((char*)P.out + ((size_t)(b * 32 + r) * 1024 + pi * 512 + 32 * si + 8 * w + 4 * h) * 2) = ov;
  if (w == 0 && lane == 0) __hip_atomic_store((GAS u64*)(ws + WS_EP + g * 256), tag, RLX_AGENT);
  STAMP(8);
  if (w == 0 && lane == 0 && (blockIdx.x % 37) == 0)
    printf("bid %3d pi %d b %d kvh %d: t0 %llu x %.2f q %.2f go %.2f loopend %.2f bo %.2f pub %.2f end %.2f clk %.3f\n", blockIdx.x, pi, b, kvh, rs_[0] % 1000000ull,
           (rs_[1] - rs_[0]) / 100.0, (rs_[2] - rs_[0]) / 100.0, (rs_[3] - rs_[0]) / 100.0, (rs_[4] - rs_[0]) / 100.0, (rs_[6] - rs_[0]) / 100.0, (rs_[7] - rs_[0]) / 100.0, (rs_[8] - rs_[0]) / 100.0,
           (double)(cs_[1] - cs_[0]) / (double)(rs_[4] - rs_[3]) / 10.0);
}

extern "C" void kernel_launch(void* const* d_in, const int* in_sizes, int n_in, void* d_out, int out_size, void* d_ws, size_t ws_size, hipStream_t stream) {
  (void)hipFuncSetAttribute((const void*)gqa_block_fused, hipFuncAttributeMaxDynamicSharedMemorySize, (int)LDS_BYTES);
  Params P;
  for (int p = 0; p < 2; ++p) { P.x[p] = d_in[5 * p + 0]; P.k[p] = d_in[5 * p + 1]; P.v[p] = d_in[5 * p + 2]; P.wq[p] = d_in[5 * p + 3]; P.wo[p] = d_in[5 * p + 4]; }
  P.out = d_out; P.ws = d_ws;
  hipLaunchKernelGGL(gqa_block_fused, dim3(256), dim3(512), LDS_BYTES, stream, P);
}
```

```cpp
#include <hip/hip_runtime.h>
#include <stdint.h>

typedef unsigned u32;
typedef unsigned long long u64;
typedef u32 u32x4 __attribute__((ext_vector_type(4)));
typedef u32 u32x2 __attribute__((ext_vector_type(2)));
typedef float f32x2 __attribute__((ext_vector_type(2)));
typedef float f32x4 __attribute__((ext_vector_type(4)));
typedef float f32x16 __attribute__((ext_vector_type(16)));
typedef __bf16 bf16x2 __attribute__((ext_vector_type(2)));
typedef __bf16 bf16x4 __attribute__((ext_vector_type(4)));
typedef __bf16 bf16x8 __attribute__((ext_vector_type(8)));
#define LAS __attribute__((address_space(3)))
#define GAS __attribute__((address_space(1)))
#define DI __device__ __forceinline__
#define RLX_AGENT __ATOMIC_RELAXED, __HIP_MEMORY_SCOPE_AGENT
#define SBAR() __builtin_amdgcn_sched_barrier(0)
#define PIN(x) asm volatile("" : "+v"(x))

constexpr u32 BUFB = 32768, EXA = 98304, EXB = 131072, LDS_BYTES = 163840;
constexpr u32 OSCR = 0, MISC = 16384;
constexpr int NT = 16;
constexpr u32 WS_EP = 0;
constexpr u32 WS_FO = 4096;
constexpr u32 WS_F3 = WS_FO + 32768;
constexpr u32 OXH = 8192 + 1024;
constexpr u32 WS_OX = 131072;
constexpr u32 WS_SL = WS_OX + 256u * 2u * OXH;
constexpr u32 WS_END = WS_SL + 16u * 16u * 16u * 2048u;
constexpr u32 WS_DBG = WS_END;
constexpr u64 TAGC = 0x9E3779B97F4A7C15ull;
constexpr float CSC = 0.088388347648318447f * 1.4426950408889634f;
constexpr float THR = 8.0f;
constexpr float LIM = 1024.f;
constexpr int AUX_SC1 = 16, AUX_NT = 2;
constexpr u32 SPIN_CAP = 1u << 18;

struct Params {
  const void* x[2]; const void* k[2]; const void* v[2]; const void* wq[2]; const void* wo[2];
  void* out; void* ws;
};

#define BAR() asm volatile("s_waitcnt lgkmcnt(0)\n\ts_barrier" ::: "memory")
#define BARN() asm volatile("s_waitcnt lgkmcnt(8)\n\ts_barrier" ::: "memory")
#define DRAIN() asm volatile("s_waitcnt vmcnt(0)" ::: "memory")
#define LGKM0() asm volatile("s_waitcnt lgkmcnt(0)" ::: "memory")
template <int N> DI void waitvm() { asm volatile("s_waitcnt vmcnt(%0)" ::"i"(N) : "memory"); }

DI void dma(u32 m0v, u32 voff, __amdgpu_buffer_rsrc_t srd, u32 soff) {
  asm volatile("s_mov_b32 m0, %0\n\ts_nop 0\n\tbuffer_load_dwordx4 %1, %2, %3 offen lds" ::"s"(m0v), "v"(voff), "s"(srd), "s"(soff) : "memory");
}
DI u32 cvtpk(float lo, float hi) { f32x2 v = {lo, hi}; bf16x2 b = __builtin_convertvector(v, bf16x2); return __builtin_bit_cast(u32, b); }
DI bf16x8 pack8(const f32x16& x, int s) {
  u32x4 w = {cvtpk(x[8 * s], x[8 * s + 1]), cvtpk(x[8 * s + 2], x[8 * s + 3]), cvtpk(x[8 * s + 4], x[8 * s + 5]), cvtpk(x[8 * s + 6], x[8 * s + 7])};
  return __builtin_bit_cast(bf16x8, w);
}
DI bf16x8 lds128(u32 a) { return *(const LAS bf16x8*)(size_t)a; }
DI void lds_st128(u32 a, bf16x8 v) { *(LAS bf16x8*)(size_t)a = v; }
DI void lds_st128u(u32 a, u32x4 v) { *(LAS u32x4*)(size_t)a = v; }
DI bf16x8 trfrag(u32 a0, u32 a1) {
  bf16x4 lo = __builtin_amdgcn_ds_read_tr16_b64_v4bf16((LAS bf16x4*)(size_t)a0);
  bf16x4 hi = __builtin_amdgcn_ds_read_tr16_b64_v4bf16((LAS bf16x4*)(size_t)a1);
  return __builtin_shufflevector(lo, hi, 0, 1, 2, 3, 4, 5, 6, 7);
}
#define MFMA(a, b, c) __builtin_amdgcn_mfma_f32_32x32x16_bf16((a), (b), (c), 0, 0, 0)
DI float swap_max(float v) { auto r = __builtin_amdgcn_permlane32_swap(__float_as_uint(v), __float_as_uint(v), false, false); return fmaxf(__uint_as_float(r[0]), __uint_as_float(r[1])); }
DI float swap_add(float v) { auto r = __builtin_amdgcn_permlane32_swap(__float_as_uint(v), __float_as_uint(v), false, false); return __uint_as_float(r[0]) + __uint_as_float(r[1]); }

struct Chk { u32 a, b; };
DI void chk_add(Chk& c, u32 d) { c.a = __builtin_amdgcn_alignbit(c.a, c.a, 27) ^ d; c.b = __builtin_amdgcn_alignbit(c.b, c.b, 19) + d; }
DI void chk_add4(Chk& c, u32x4 v) { chk_add(c, v[0]); chk_add(c, v[1]); chk_add(c, v[2]); chk_add(c, v[3]); }
DI Chk chk_init(u64 tag) { return Chk{(u32)tag, (u32)(tag >> 32)}; }
DI u32x4 granule(u64 tag, u32 d0, u32 d1) { Chk c = chk_init(tag); chk_add(c, d0); chk_add(c, d1); chk_add(c, d0 ^ d1); return u32x4{d0, d1, c.a, c.b}; }
DI bool granule_ok(u64 tag, u32x4 v) { const u32x4 e = granule(tag, v[0], v[1]); return e[2] == v[2] && e[3] == v[3]; }
DI void poll_flags(const GAS u64* base, int mask, int lane, u64 tag) {
  const GAS u64* p = base + (lane & mask);
#pragma unroll 1
  for (u32 spins = 0; spins < SPIN_CAP; ++spins) {
    u64 v = __hip_atomic_load(p, RLX_AGENT);
    if (__all(v == tag)) break;
    __builtin_amdgcn_s_sleep(1);
  }
  asm volatile("" ::: "memory");
}
#define STAMP(i) do {} while (0)

#define PADN -1
DI void pad() { if constexpr (PADN >= 0) asm volatile("s_nop %0" :: "i"(PADN >= 0 ? PADN : 0)); }
struct St {
  bf16x8 qf[8], kf[8], vf[8];
  f32x16 o[4], sA, sB;
  u32x4 pA[2], pB[2];
  float e[2][2];
  float bs[2];
  float l, mc;
  u32 ak[2], av[2];
};
template <int G> DI void sm_slice(St& c, const f32x16& s, u32x4 (&p)[2]) {
  c.e[(G >> 1) & 1][G & 1] = __builtin_amdgcn_exp2f(fmaf(s[G], CSC, -c.mc));
  if constexpr (G >= 1) { constexpr int H = G - 1; c.bs[H & 1] += c.e[(H >> 1) & 1][H & 1]; PIN(c.bs[H & 1]); }
  if constexpr (G >= 2 && (G & 1) == 0) { constexpr int Q = (G - 2) >> 1; u32 w = cvtpk(c.e[Q & 1][0], c.e[Q & 1][1]); PIN(w); p[Q >> 2][Q & 3] = w; }
}
template <int I> DI bf16x8 ldk1(u32 a0, u32 a1, int kb) { return lds128(((I & 1) ? a1 : a0) + 8192u * kb + 512u * (I >> 1)); }
template <int I> DI bf16x8 ldv1(u32 a0, u32 a1, int kb) { return trfrag(a0 + 8192u * kb + 4096u * (I >> 2) + 512u * (I & 3), a1 + 8192u * kb + 4096u * (I >> 2) + 512u * (I & 3)); }
DI bool sm_finish(St& c, u32x4 (&p)[2]) {
  c.bs[1] += c.e[1][1];
  p[1][3] = cvtpk(c.e[1][0], c.e[1][1]);
  const float bsum = c.bs[0] + c.bs[1];
  if (__builtin_expect(__any(!(bsum <= LIM)), 0)) return false;
  c.l += bsum; c.bs[0] = 0.f; c.bs[1] = 0.f;
  return true;
}
template <int I, bool QK, bool LD, bool SM> DI void qk_gap(St& c, f32x16& snew, const f32x16& sold, u32x4 (&p)[2], u32 a0, u32 a1, int kb) {
  if constexpr (QK) { if constexpr (I == 0) snew = MFMA(c.kf[0], c.qf[0], f32x16{}); else snew = MFMA(c.kf[I], c.qf[I], snew); PIN(snew); }
  if constexpr (LD) c.kf[I] = ldk1<I>(a0, a1, kb);
  if constexpr (SM) sm_slice<8 + I>(c, sold, p);
  pad();
  SBAR();
}
template <int I, bool PV, bool LD, bool SM> DI void pv_gap(St& c, const u32x4 (&pc)[2], const f32x16& s, u32x4 (&pn)[2], u32 a0, u32 a1, int kb) {
  if constexpr (PV) { c.o[I & 3] = MFMA(c.vf[I], __builtin_bit_cast(bf16x8, pc[I >> 2]), c.o[I & 3]); PIN(c.o[I & 3]); }
  if constexpr (LD) c.vf[I] = ldv1<I>(a0, a1, kb);
  if constexpr (SM) sm_slice<I>(c, s, pn);
  pad();
  SBAR();
}
#define G8(F, ...) F<0 __VA_ARGS__; F<1 __VA_ARGS__; F<2 __VA_ARGS__; F<3 __VA_ARGS__; F<4 __VA_ARGS__; F<5 __VA_ARGS__; F<6 __VA_ARGS__; F<7 __VA_ARGS__;
DI void slow_block(St& c, u32 buf, int kb) {
  f32x16 s = f32x16{};
  const u32 a0 = buf + c.ak[0] + 8192u * kb, a1 = buf + c.ak[1] + 8192u * kb, v0 = buf + c.av[0] + 8192u * kb, v1 = buf + c.av[1] + 8192u * kb;
#pragma unroll
  for (int i = 0; i < 8; ++i) s = MFMA(lds128(((i & 1) ? a1 : a0) + 512u * (i >> 1)), c.qf[i], s);
  float mx = s[0];
#pragma unroll
  for (int i = 1; i < 16; ++i) mx = fmaxf(mx, s[i]);
  const float mn = fmaxf(c.mc, swap_max(mx) * CSC);
  const float al = __builtin_amdgcn_exp2f(c.mc - mn);
  c.mc = mn;
  float bsum = 0.f;
#pragma unroll
  for (int i = 0; i < 16; ++i) { s[i] = __builtin_amdgcn_exp2f(fmaf(s[i], CSC, -mn)); bsum += s[i]; }
  c.l = c.l * al + bsum;
#pragma unroll
  for (int d = 0; d < 4; ++d)
#pragma unroll
    for (int i = 0; i < 16; ++i) c.o[d][i] *= al;
  const bf16x8 p0 = pack8(s, 0), p1 = pack8(s, 1);
#pragma unroll
  for (int i = 0; i < 8; ++i) c.o[i & 3] = MFMA(trfrag(v0 + 4096u * (i >> 2) + 512u * (i & 3), v1 + 4096u * (i >> 2) + 512u * (i & 3)), (i >> 2) ? p1 : p0, c.o[i & 3]);
}
DI u32 ring(u32 L0, int t) { return L0 + (u32)(t % 3) * BUFB; }

extern "C" __global__ __launch_bounds__(512, 2) void gqa_block_fused(Params P) {
  extern __shared__ __attribute__((aligned(16))) char lds_[];
  const int tid = threadIdx.x, lane = tid & 63;
  const int wave = __builtin_amdgcn_readfirstlane(tid >> 6);
  const int bid = blockIdx.x;
  const int cidx = (bid & 7) * 4 + (bid >> 6), b = (bid >> 3) & 7;
  const int pi = cidx >> 4, kvh = (cidx >> 1) & 7, sp = cidx & 1;
  const int g = pi * 8 + b, si = kvh * 2 + sp, wg = g * 16 + si;
  const u32 L0 = (u32)(size_t)lds_;
  char* ws = (char*)P.ws;
  const int h = lane >> 5, r = lane & 31, blk = (lane >> 4) & 1, q4 = (lane >> 2) & 3, p4 = lane & 3;
  const __amdgpu_buffer_rsrc_t sW = __builtin_amdgcn_make_buffer_rsrc(ws, 0, (int)WS_END, 0x00020000);
  STAMP(0);

  if (wave >= 4) {
    const int w = wave - 4;
    const size_t kvo = ((size_t)(b * 8 + kvh) * 2048 + (size_t)sp * 1024) * 256;
    const __amdgpu_buffer_rsrc_t sKV = __builtin_amdgcn_make_buffer_rsrc((char*)(w < 2 ? P.k[pi] : P.v[pi]) + kvo, 0, 262144, 0x00020000);
    const __amdgpu_buffer_rsrc_t sO = __builtin_amdgcn_make_buffer_rsrc((char*)P.wo[pi] + (size_t)(kvh * 4 + sp * 2) * 128 * 1024, 0, 262144, 0x00020000);
    u32 vk[2], vo[2];
#pragma unroll
    for (int i = 0; i < 2; ++i) {
      vk[i] = 256u * (u32)((lane >> 2) & 7) + 64u * (u32)h + 16u * ((u32)(lane & 3) ^ (u32)(2 * i + ((lane >> 4) & 1))) + 8192u * (u32)(w & 1);
      const u32 rm = (2 * i + h) & 3, ch = (u32)r ^ (rm << 2);
      vo[i] = (u32)h * 1024u + 256u * (ch >> 3) + 16u * (ch & 7u) + 128u;
    }
    const u32 vw = (u32)(lane >> 3) * 1024u + 16u * ((u32)(lane & 7) ^ (u32)(((lane >> 4) & 1) << 2)) + 256u * (u32)w;
    u32x4 st[6][8];
    auto load_item = [&](int i) {
      if (i < NT) {
#pragma unroll
        for (int j = 0; j < 8; ++j) st[i % 6][j] = __builtin_amdgcn_raw_buffer_load_b128(sKV, vk[(j >> 1) & 1] + 2048u * ((j >> 1) & 1) + 128u * (j & 1), (u32)i * 16384u + 4096u * (j >> 2), AUX_NT);
      } else {
        const u32 c = (u32)(i - NT + b) & 3u;
#pragma unroll
        for (int j = 0; j < 8; ++j) st[i % 6][j] = __builtin_amdgcn_raw_buffer_load_b128(sO, vw, 65536u * c + 8192u * j, AUX_NT);
      }
      SBAR();
    };
    u32 wb = L0 + 8192u * w + 16u * lane;
    asm volatile("" : "+v"(wb));
    auto write_tile = [&](int i) {
#pragma unroll
      for (int j = 0; j < 8; ++j) lds_st128u(wb + (u32)(i % 3) * BUFB + 1024u * j, st[i % 6][j]);
      SBAR();
    };
    bf16x8 wr[2][16];
    const u32 aw0 = L0 + EXA + 8192u * w + 128u * (4u * h + q4) + 16u * ((2u * blk + (p4 >> 1)) ^ (u32)((q4 >> 1) << 2)) + 8u * (p4 & 1);
    auto conv_w = [&](int cc) {
#pragma unroll
      for (int j = 0; j < 8; ++j) lds_st128u(wb + EXA + 1024u * j, st[(NT + cc) % 6][j]);
      LGKM0();
#pragma unroll
      for (int kk = 0; kk < 4; ++kk)
#pragma unroll
        for (int T = 0; T < 2; ++T) { const u32 a = (aw0 ^ (64u * T)) + 2048u * kk; wr[T][4 * cc + kk] = trfrag(a, a + 1024u); }
      LGKM0();
      SBAR();
    };
    auto issue_wol = [&](int cc, u32 dst) {
      const u32 c = (u32)(cc + b) & 3u;
#pragma unroll
      for (int i = 0; i < 8; ++i) { const u32 I = 8u * w + i; dma(L0 + dst + 1024u * I, vo[i & 1], sO, (64u * c + 2u * I) * 1024u); }
    };
    BAR();
    load_item(0);
    BAR(); BAR();
    load_item(1); load_item(2); load_item(3); load_item(4); load_item(5);
    BAR();
    write_tile(0); load_item(6); write_tile(1); load_item(7); write_tile(2); load_item(8);
    BAR(); BAR(); BAR();
#pragma unroll
    for (int u = 3; u < NT; ++u) {
      write_tile(u);
      if (u + 6 < NT) load_item(u + 6);
      if (u >= 12) load_item(NT + u - 12);
      if (u == 12) issue_wol(0, EXB);
      if (u >= 14) conv_w(u - 14);
      BAR();
    }
    issue_wol(1, BUFB * (13 % 3));
    conv_w(2);
    BAR();
    issue_wol(3, BUFB * (14 % 3));
    conv_w(3);
    issue_wol(2, EXA);
    STAMP(5);
    DRAIN(); BAR();
    STAMP(6);
    const u64 tag = *(volatile LAS u64*)(size_t)(L0 + MISC);
    f32x16 acc[2] = {f32x16{}, f32x16{}};
    {
      bf16x8 B[16];
#pragma unroll
      for (int j = 0; j < 16; ++j) B[j] = lds128(L0 + OSCR + 1024u * (4u * ((u32)((j >> 2) + b) & 3u) + (j & 3)) + 16u * lane);
#pragma unroll
      for (int j = 0; j < 16; ++j) { acc[0] = MFMA(wr[0][j], B[j], acc[0]); acc[1] = MFMA(wr[1][j], B[j], acc[1]); }
    }
#pragma unroll
    for (int n = 0; n < 2; ++n)
#pragma unroll
      for (int u = 0; u < 2; ++u)
        __builtin_amdgcn_raw_buffer_store_b128(__builtin_bit_cast(u32x4, pack8(acc[n], u)), sW, 1024u * u + 16u * lane + WS_SL + (u32)((g * 16 + 4 * w + n) * 16 + si) * 2048u, 0, AUX_SC1);
    DRAIN();
    if (lane == 0) __hip_atomic_store((GAS u64*)(ws + WS_F3 + (g * 8 + wave) * 128) + si, tag, RLX_AGENT);
    STAMP(7);
    return;
  }

  const int w = wave;
  St c;
  u64 tag;
  {
    const __amdgpu_buffer_rsrc_t sX = __builtin_amdgcn_make_buffer_rsrc((char*)P.x[pi] + (size_t)b * 32768, 0, 32768, 0x00020000);
    const __amdgpu_buffer_rsrc_t sQ = __builtin_amdgcn_make_buffer_rsrc((char*)P.wq[pi] + (size_t)(kvh * 512) * 2, 0, 511 * 8192 + 1024, 0x00020000);
    const u32 vq = (u32)(lane >> 4) * 8192u + 16u * ((u32)(lane & 15) ^ (u32)((lane >> 4) << 2)) + 256u * (u32)w;
    auto issue_wq = [&](int cc) {
      const u32 ch = (u32)(cc + 2 * b) & 15u;
#pragma unroll
      for (int i = 0; i < 8; ++i) dma(L0 + 32768u * w + 8192u * (cc & 3) + 1024u * i, vq, sQ, (32u * ch + 4u * i) * 8192u);
    };
    {
      const GAS u64* ep = (const GAS u64*)(ws + WS_EP + g * 256);
      asm volatile("global_load_dwordx2 %0, %1, off sc1" : "=v"(tag) : "v"(ep) : "memory");
    }
#pragma unroll
    for (int i = 0; i < 8; ++i) { const u32 q = 8u * w + i; dma(L0 + EXB + 1024u * q, (16u * lane) ^ (16u * (q & 15u)), sX, 1024u * q); }
    issue_wq(0); issue_wq(1); issue_wq(2); issue_wq(3);
    BAR();
    waitvm<32>(); BAR();
    asm volatile("" : "+v"(tag));
    tag += TAGC;
    STAMP(1);
    bf16x8 xf[32];
#pragma unroll
    for (int j = 0; j < 32; ++j) {
      const u32 ks = 2u * ((u32)((j >> 1) + 2 * b) & 15u) + (j & 1);
      xf[j] = lds128(L0 + EXB + 1024u * r + ((32u * ks + 16u * h) ^ (16u * (r & 15))));
    }
    BAR();
    f32x16 qa[4] = {f32x16{}, f32x16{}, f32x16{}, f32x16{}};
    const u32 aq0 = L0 + 32768u * w + 256u * (8u * h + q4) + 64u * q4 + 16u * (2u * blk + (p4 & 1)) + 8u * (p4 >> 1);
#pragma unroll
    for (int cc = 0; cc < 16; ++cc) {
      if (cc <= 12) waitvm<24>(); else if (cc == 13) waitvm<16>(); else if (cc == 14) waitvm<8>(); else waitvm<0>();
      bf16x8 af[8];
#pragma unroll
      for (int kk = 0; kk < 2; ++kk)
#pragma unroll
        for (int T = 0; T < 4; ++T) { const u32 a = (aq0 ^ (64u * T)) + 8192u * (cc & 3) + 4096u * kk; af[4 * kk + T] = trfrag(a, a + 1024u); }
      LGKM0();
      if (cc < 12) issue_wq(cc + 4);
      SBAR();
#pragma unroll
      for (int kk = 0; kk < 2; ++kk)
#pragma unroll
        for (int T = 0; T < 4; ++T) qa[T] = MFMA(af[4 * kk + T], xf[2 * cc + kk], qa[T]);
    }
    STAMP(2);
#pragma unroll
    for (int T = 0; T < 4; ++T) { c.qf[2 * T] = pack8(qa[T], 0); c.qf[2 * T + 1] = pack8(qa[T], 1); }
  }
  const bool own = (w >> 1) == sp;
  const int hl = w & 1;
  BAR();
  STAMP(3);
#pragma unroll
  for (int e = 0; e < 2; ++e) {
    c.ak[e] = 2048u * (u32)(r >> 3) + 64u * (u32)(r & 7) + 16u * ((u32)(2 * e + h) ^ (u32)((r >> 2) & 3));
    c.av[e] = 16384u + 2048u * e + 64u * (u32)(4 * h + q4) + 16u * ((u32)(2 * blk + (p4 >> 1)) ^ (u32)((2 * e + h) & 3)) + 8u * (u32)(p4 & 1);
  }
#pragma unroll
  for (int d = 0; d < 4; ++d) c.o[d] = f32x16{};
  c.l = 0.f; c.bs[0] = c.bs[1] = 0.f;
  BAR();
  {
    u32 a0 = L0 + c.ak[0], a1 = L0 + c.ak[1], v0 = L0 + c.av[0], v1 = L0 + c.av[1];
    asm("" : "+v"(a0)); asm("" : "+v"(a1)); asm("" : "+v"(v0)); asm("" : "+v"(v1));
#pragma unroll
    for (int i = 0; i < 8; ++i) c.kf[i] = lds128(((i & 1) ? a1 : a0) + 512u * (i >> 1));
    G8(qk_gap, , true, true, false>(c, c.sA, c.sA, c.pA, a0, a1, 1))
    float mx = c.sA[0];
#pragma unroll
    for (int i = 1; i < 16; ++i) mx = fmaxf(mx, c.sA[i]);
    c.mc = swap_max(mx) * CSC;
    G8(pv_gap, , false, true, true>(c, c.pA, c.sA, c.pA, v0, v1, 0))
  }
  int slow_t = NT, slow_kb = 0;
  {
    int t = 0;
    u32 bc = 0, bn = BUFB;
    for (; t < NT - 1; ++t) {
      u32 kn0 = L0 + bn + c.ak[0], kn1 = L0 + bn + c.ak[1], vc0 = L0 + bc + c.av[0], vc1 = L0 + bc + c.av[1], vn0 = L0 + bn + c.av[0], vn1 = L0 + bn + c.av[1];
      asm("" : "+v"(kn0)); asm("" : "+v"(kn1)); asm("" : "+v"(vc0)); asm("" : "+v"(vc1)); asm("" : "+v"(vn0)); asm("" : "+v"(vn1));
      BARN();
      G8(qk_gap, , true, true, true>(c, c.sB, c.sA, c.pA, kn0, kn1, 0))
      if (!sm_finish(c, c.pA)) { slow_t = t; slow_kb = 0; break; }
      G8(pv_gap, , true, true, true>(c, c.pA, c.sB, c.pB, vc0, vc1, 1))
      G8(qk_gap, , true, true, true>(c, c.sA, c.sB, c.pB, kn0, kn1, 1))
      if (!sm_finish(c, c.pB)) { slow_t = t; slow_kb = 1; break; }
      G8(pv_gap, , true, true, true>(c, c.pB, c.sA, c.pA, vn0, vn1, 0))
      bc = bn; bn = (bn == 2 * BUFB) ? 0u : bn + BUFB;
    }
    if (slow_t == NT) {
      u32 vc0 = L0 + bc + c.av[0], vc1 = L0 + bc + c.av[1];
      G8(qk_gap, , true, false, true>(c, c.sB, c.sA, c.pA, 0u, 0u, 0))
      if (!sm_finish(c, c.pA)) { slow_t = NT - 1; slow_kb = 0; }
      else {
        G8(pv_gap, , true, true, true>(c, c.pA, c.sB, c.pB, vc0, vc1, 1))
        G8(qk_gap, , false, false, true>(c, c.sA, c.sB, c.pB, 0u, 0u, 0))
        if (!sm_finish(c, c.pB)) { slow_t = NT - 1; slow_kb = 1; }
        else {
          BAR();
          G8(pv_gap, , true, false, false>(c, c.pB, c.sA, c.pA, 0u, 0u, 0))
        }
      }
    }
  }
  if (__builtin_expect(slow_t < NT, 0)) {
#pragma unroll 1
    for (int t = slow_t; t < NT; ++t) {
      if (t > slow_t && t < NT - 1) BAR();
      const u32 buf = ring(L0, t);
      if (t > slow_t || slow_kb == 0) slow_block(c, buf, 0);
      slow_block(c, buf, 1);
    }
    BAR();
  }
  const float l = swap_add(c.l), mc = c.mc;
  STAMP(4);
  if (!own) {
    const u32 ob = WS_OX + (u32)(wg * 2 + hl) * OXH;
    const float inv = 1.0f / l;
    Chk ck = chk_init(tag);
#pragma unroll
    for (int d = 0; d < 4; ++d) {
#pragma unroll
      for (int i = 0; i < 16; ++i) c.o[d][i] *= inv;
#pragma unroll
      for (int s2 = 0; s2 < 2; ++s2) {
        const u32x4 f = __builtin_bit_cast(u32x4, pack8(c.o[d], s2));
        __builtin_amdgcn_raw_buffer_store_b128(f, sW, 1024u * (2 * d + s2) + 16u * lane + ob, 0, AUX_SC1);
        chk_add4(ck, f);
      }
    }
    chk_add(ck, __float_as_uint(mc)); chk_add(ck, __float_as_uint(l));
    __builtin_amdgcn_raw_buffer_store_b128(u32x4{__float_as_uint(mc), __float_as_uint(l), ck.a, ck.b}, sW, 8192u + 16u * lane + ob, 0, AUX_SC1);
    if (lane == 0) __hip_atomic_store((GAS u64*)(ws + WS_FO + wg * 128) + hl, tag, RLX_AGENT);
    if (hl == 0 && lane == 0) *(volatile LAS u64*)(size_t)(L0 + MISC) = tag;
  } else {
    const u32 ob = WS_OX + (u32)((wg ^ 1) * 2 + hl) * OXH;
    u32x4 f[8], st;
    poll_flags((const GAS u64*)(ws + WS_FO + (wg ^ 1) * 128) + hl, 0, lane, tag);
#pragma unroll 1
    for (u32 spins = 0; spins < SPIN_CAP; ++spins) {
#pragma unroll
      for (int j = 0; j < 8; ++j) f[j] = __builtin_amdgcn_raw_buffer_load_b128(sW, 1024u * j + 16u * lane, ob, AUX_SC1);
      st = __builtin_amdgcn_raw_buffer_load_b128(sW, 8192u + 16u * lane, ob, AUX_SC1);
      Chk c2 = chk_init(tag);
#pragma unroll
      for (int j = 0; j < 8; ++j) chk_add4(c2, f[j]);
      chk_add(c2, st[0]); chk_add(c2, st[1]);
      if (__all(c2.a == st[2] && c2.b == st[3])) break;
      __builtin_amdgcn_s_sleep(2);
    }
    const float m2 = __uint_as_float(st[0]), lp = __uint_as_float(st[1]);
    const float mn = fmaxf(mc, m2);
    const float a1 = __builtin_amdgcn_exp2f(mc - mn), a2 = __builtin_amdgcn_exp2f(m2 - mn) * lp;
    const float inv = 1.0f / (l * a1 + a2);
    const float f1 = a1 * inv, f2 = a2 * inv;
#pragma unroll
    for (int j = 0; j < 8; ++j) {
      u32x4 o4;
#pragma unroll
      for (int e = 0; e < 4; ++e) {
        const float lo = c.o[j >> 1][8 * (j & 1) + 2 * e] * f1 + __uint_as_float(f[j][e] << 16) * f2;
        const float hi = c.o[j >> 1][8 * (j & 1) + 2 * e + 1] * f1 + __uint_as_float(f[j][e] & 0xffff0000u) * f2;
        o4[e] = cvtpk(lo, hi);
      }
      lds_st128u(L0 + OSCR + (u32)(hl * 8 + j) * 1024u + 16u * lane, o4);
    }
  }
  STAMP(5);
  BAR();
  STAMP(6);
  {
    f32x16 acc[2] = {f32x16{}, f32x16{}};
    const u32 ao0 = L0 + 512u * (4u * h + q4) + 16u * ((8u * w + 2u * blk + (p4 >> 1)) ^ (4u * q4)) + 8u * (p4 & 1);
#pragma unroll
    for (int cc = 0; cc < 4; ++cc) {
      bf16x8 B[4], A[8];
      const u32 buf = cc == 0 ? EXB : cc == 1 ? BUFB * (13 % 3) : cc == 2 ? EXA : BUFB * (14 % 3);
#pragma unroll
      for (int kk = 0; kk < 4; ++kk) {
        B[kk] = lds128(L0 + OSCR + 1024u * (4u * ((u32)(cc + b) & 3u) + kk) + 16u * lane);
#pragma unroll
        for (int T = 0; T < 2; ++T) { const u32 a = buf + (ao0 ^ (64u * T)) + 8192u * kk; A[2 * kk + T] = trfrag(a, a + 4096u); }
      }
#pragma unroll
      for (int kk = 0; kk < 4; ++kk) { acc[0] = MFMA(A[2 * kk], B[kk], acc[0]); acc[1] = MFMA(A[2 * kk + 1], B[kk], acc[1]); }
    }
#pragma unroll
    for (int n = 0; n < 2; ++n)
#pragma unroll
      for (int u = 0; u < 2; ++u)
        __builtin_amdgcn_raw_buffer_store_b128(__builtin_bit_cast(u32x4, pack8(acc[n], u)), sW, 1024u * u + 16u * lane + WS_SL + (u32)((g * 16 + 4 * w + 2 + n) * 16 + si) * 2048u, 0, AUX_SC1);
    DRAIN();
    if (lane == 0) __hip_atomic_store((GAS u64*)(ws + WS_F3 + (g * 8 + wave) * 128) + si, tag, RLX_AGENT);
  }
  STAMP(7);
  poll_flags((const GAS u64*)(ws + WS_F3 + (g * 8 + (si >> 2) + ((si & 2) ? 0 : 4)) * 128), 15, lane, tag);
  f32x4 sum = {0.f, 0.f, 0.f, 0.f};
  {
    u32x2 v[16];
#pragma unroll
    for (int i = 0; i < 16; ++i)
      v[i] = __builtin_amdgcn_raw_buffer_load_b64(sW, 1024u * (w >> 1) + 16u * lane + 8u * (w & 1) + 2048u * i, WS_SL + (u32)((g * 16 + si) * 16) * 2048u, AUX_SC1);
#pragma unroll
    for (int i = 0; i < 16; ++i) {
      sum[0] += __uint_as_float(v[i][0] << 16); sum[1] += __uint_as_float(v[i][0] & 0xffff0000u);
      sum[2] += __uint_as_float(v[i][1] << 16); sum[3] += __uint_as_float(v[i][1] & 0xffff0000u);
    }
  }
  u32x2 ov = {cvtpk(sum[0], sum[1]), cvtpk(sum[2], sum[3])};
  *(GAS u32x2*)((char*)P.out + ((size_t)(b * 32 + r) * 1024 + pi * 512 + 32 * si + 8 * w + 4 * h) * 2) = ov;
  if (w == 0 && lane == 0) __hip_atomic_store((GAS u64*)(ws + WS_EP + g * 256), tag, RLX_AGENT);
  STAMP(8);
}

extern "C" void kernel_launch(void* const* d_in, const int* in_sizes, int n_in, void* d_out, int out_size, void* d_ws, size_t ws_size, hipStream_t stream) {
  (void)hipFuncSetAttribute((const void*)gqa_block_fused, hipFuncAttributeMaxDynamicSharedMemorySize, (int)LDS_BYTES);
  Params P;
  for (int p = 0; p < 2; ++p) { P.x[p] = d_in[5 * p + 0]; P.k[p] = d_in[5 * p + 1]; P.v[p] = d_in[5 * p + 2]; P.wq[p] = d_in[5 * p + 3]; P.wo[p] = d_in[5 * p + 4]; }
  P.out = d_out; P.ws = d_ws;
  hipLaunchKernelGGL(gqa_block_fused, dim3(256), dim3(512), LDS_BYTES, stream, P);
}
```

```cpp
#include <hip/hip_runtime.h>
#include <stdint.h>
#include <type_traits>

typedef unsigned u32;
typedef unsigned long long u64;
typedef u32 u32x4 __attribute__((ext_vector_type(4)));
typedef u32 u32x2 __attribute__((ext_vector_type(2)));
typedef float f32x2 __attribute__((ext_vector_type(2)));
typedef float f32x4 __attribute__((ext_vector_type(4)));
typedef float f32x16 __attribute__((ext_vector_type(16)));
typedef __bf16 bf16x2 __attribute__((ext_vector_type(2)));
typedef __bf16 bf16x4 __attribute__((ext_vector_type(4)));
typedef __bf16 bf16x8 __attribute__((ext_vector_type(8)));
#define LAS __attribute__((address_space(3)))
#define GAS __attribute__((address_space(1)))
#define DI __device__ __forceinline__
#define RLX_AGENT __ATOMIC_RELAXED, __HIP_MEMORY_SCOPE_AGENT
#define SBAR() __builtin_amdgcn_sched_barrier(0)
#define PIN(x) asm volatile("" : "+v"(x))

constexpr u32 KR = 0, VR = 65536, TB = 16384, EX = 131072, LDS_BYTES = 163840;
constexpr u32 MS = 98304;
constexpr u32 MISC = MS + 6144;
constexpr int NT = 32, NREG = 6;
constexpr u32 WS_EP = 0;
constexpr u32 WS_F3 = 4096;
constexpr u32 WS_SL = 32768;
constexpr u32 WS_END = WS_SL + 16u * 16u * 16u * 2048u;
constexpr u32 WS_DBG = WS_END;
constexpr u64 TAGC = 0x9E3779B97F4A7C15ull;
constexpr float CSC = 0.088388347648318447f * 1.4426950408889634f;
constexpr float THR = 8.0f;
constexpr int AUX_SC1 = 16, AUX_NT = 0;
constexpr u32 SPIN_CAP = 1u << 18;

struct Params {
  const void* x[2]; const void* k[2]; const void* v[2]; const void* wq[2]; const void* wo[2];
  void* out; void* ws;
};

#define BAR() asm volatile("s_waitcnt lgkmcnt(0)\n\ts_barrier" ::: "memory")
#define DRAIN() asm volatile("s_waitcnt vmcnt(0)" ::: "memory")
#define LGKM0() asm volatile("s_waitcnt lgkmcnt(0)" ::: "memory")
template <int N> DI void waitvm() { asm volatile("s_waitcnt vmcnt(%0)" ::"i"(N) : "memory"); }

DI void dma(u32 m0v, u32 voff, __amdgpu_buffer_rsrc_t srd, u32 soff) {
  asm volatile("s_mov_b32 m0, %0\n\ts_nop 0\n\tbuffer_load_dwordx4 %1, %2, %3 offen lds" ::"s"(m0v), "v"(voff), "s"(srd), "s"(soff) : "memory");
}
DI u32 cvtpk(float lo, float hi) { f32x2 v = {lo, hi}; bf16x2 b = __builtin_convertvector(v, bf16x2); return __builtin_bit_cast(u32, b); }
DI bf16x8 pack8(const f32x16& x, int s) {
  u32x4 w = {cvtpk(x[8 * s], x[8 * s + 1]), cvtpk(x[8 * s + 2], x[8 * s + 3]), cvtpk(x[8 * s + 4], x[8 * s + 5]), cvtpk(x[8 * s + 6], x[8 * s + 7])};
  return __builtin_bit_cast(bf16x8, w);
}
DI bf16x8 lds128(u32 a) { return *(const LAS bf16x8*)(size_t)a; }
DI u32x4 lds128u(u32 a) { return *(const LAS u32x4*)(size_t)a; }
DI void lds_st128(u32 a, bf16x8 v) { *(LAS bf16x8*)(size_t)a = v; }
DI void lds_st128u(u32 a, u32x4 v) { *(LAS u32x4*)(size_t)a = v; }
DI bf16x8 trfrag(u32 a0, u32 a1) {
  bf16x4 lo = __builtin_amdgcn_ds_read_tr16_b64_v4bf16((LAS bf16x4*)(size_t)a0);
  bf16x4 hi = __builtin_amdgcn_ds_read_tr16_b64_v4bf16((LAS bf16x4*)(size_t)a1);
  return __builtin_shufflevector(lo, hi, 0, 1, 2, 3, 4, 5, 6, 7);
}
#define MFMA(a, b, c) __builtin_amdgcn_mfma_f32_32x32x16_bf16((a), (b), (c), 0, 0, 0)
DI float swap_max(float v) { auto r = __builtin_amdgcn_permlane32_swap(__float_as_uint(v), __float_as_uint(v), false, false); return fmaxf(__uint_as_float(r[0]), __uint_as_float(r[1])); }
DI float swap_add(float v) { auto r = __builtin_amdgcn_permlane32_swap(__float_as_uint(v), __float_as_uint(v), false, false); return __uint_as_float(r[0]) + __uint_as_float(r[1]); }
DI float max3(float a, float b, float c) { float r; asm("v_max3_f32 %0, %1, %2, %3" : "=v"(r) : "v"(a), "v"(b), "v"(c)); return r; }

DI void poll_flags(const GAS u64* base, int mask, int lane, u64 tag) {
  const GAS u64* p = base + (lane & mask);
#pragma unroll 1
  for (u32 spins = 0; spins < SPIN_CAP; ++spins) {
    u64 v = __hip_atomic_load(p, RLX_AGENT);
    if (__all(v == tag)) break;
    __builtin_amdgcn_s_sleep(1);
  }
  asm volatile("" ::: "memory");
}
#define STAMP(i) do {} while (0)

struct St {
  bf16x8 qf[8], kf[8], vf[8];
  f32x16 o[4], sA, sB;
  u32x4 p[2];
  float e[2][2];
  float bs[2];
  float l, mc, mx, al;
  bool pend;
  u32 ak[2], av[2];
};
template <int G> DI void sm_slice(St& c, const f32x16& s) {
  c.e[(G >> 1) & 1][G & 1] = __builtin_amdgcn_exp2f(fmaf(s[G], CSC, -c.mc));
  if constexpr (G >= 1) { constexpr int H = G - 1; c.bs[H & 1] += c.e[(H >> 1) & 1][H & 1]; PIN(c.bs[H & 1]); }
  if constexpr (G >= 2 && (G & 1) == 0) { constexpr int Q = (G - 2) >> 1; u32 w = cvtpk(c.e[Q & 1][0], c.e[Q & 1][1]); PIN(w); c.p[Q >> 2][Q & 3] = w; }
}
DI void sm_tail(St& c) {
  c.bs[1] += c.e[1][1];
  c.p[1][3] = cvtpk(c.e[1][0], c.e[1][1]);
  c.l += c.bs[0] + c.bs[1];
  c.bs[0] = 0.f; c.bs[1] = 0.f;
}
template <int I> DI bf16x8 ldk1(u32 a0, u32 a1) { return lds128(((I & 1) ? a1 : a0) + 512u * (I >> 1)); }
template <int I> DI bf16x8 ldv1(u32 a0, u32 a1) { return trfrag(a0 + 4096u * (I >> 2) + 512u * (I & 3), a1 + 4096u * (I >> 2) + 512u * (I & 3)); }
template <int I, bool QK> DI void qk_gap(St& c, f32x16& snew, const f32x16& scur, u32 v0, u32 v1) {
  if constexpr (QK) { if constexpr (I == 0) snew = MFMA(c.kf[0], c.qf[0], f32x16{}); else snew = MFMA(c.kf[I], c.qf[I], snew); PIN(snew); }
  c.vf[I] = ldv1<I>(v0, v1);
  if constexpr (I < 4) { sm_slice<4 + 2 * I>(c, scur); sm_slice<5 + 2 * I>(c, scur); }
  else sm_slice<8 + I>(c, scur);
  if constexpr (I == 7) sm_tail(c);
  SBAR();
}
template <int I, bool MX> DI void pv_gap(St& c, const f32x16& s) {
  c.o[I & 3] = MFMA(c.vf[I], __builtin_bit_cast(bf16x8, c.p[I >> 2]), c.o[I & 3]); PIN(c.o[I & 3]);
  if constexpr (MX) {
    if constexpr (I == 2) {
      const float m0 = fmaxf(s[0], s[1]);
      float ma = max3(m0, s[2], s[3]), mb = max3(m0, s[4], s[5]);
      ma = max3(ma, s[6], s[7]); mb = max3(mb, s[8], s[9]);
      c.mx = ma; c.al = mb;
    } else if constexpr (I == 3) {
      float ma = max3(c.mx, s[10], s[11]), mb = max3(c.al, s[12], s[13]);
      ma = max3(ma, s[14], s[15]);
      c.mx = fmaxf(ma, mb);
    } else if constexpr (I == 4) {
      c.pend = false;
      if (__builtin_expect(__any(fmaf(c.mx, CSC, -c.mc) > THR), 0)) {
        const float mn = fmaxf(c.mc, swap_max(c.mx) * CSC);
        c.al = __builtin_amdgcn_exp2f(c.mc - mn);
        c.l *= c.al; c.mc = mn; c.pend = true;
      }
    } else if constexpr (I == 7) {
      if (__builtin_expect(c.pend, 0)) {
#pragma unroll
        for (int d = 0; d < 4; ++d)
#pragma unroll
          for (int i = 0; i < 16; ++i) c.o[d][i] *= c.al;
      }
    }
  }
  SBAR();
}
#define G8(F, ...) F<0 __VA_ARGS__; F<1 __VA_ARGS__; F<2 __VA_ARGS__; F<3 __VA_ARGS__; F<4 __VA_ARGS__; F<5 __VA_ARGS__; F<6 __VA_ARGS__; F<7 __VA_ARGS__;
template <bool NEXT> DI void tile_step(St& c, f32x16& scur, f32x16& snew, u32 L0, int t) {
  u32 k0 = L0 + KR + (u32)((t + 1) & 3) * TB + c.ak[0], k1 = L0 + KR + (u32)((t + 1) & 3) * TB + c.ak[1];
  u32 v0 = L0 + VR + (u32)(t & 3) * TB + c.av[0], v1 = L0 + VR + (u32)(t & 3) * TB + c.av[1];
  asm("" : "+v"(k0)); asm("" : "+v"(k1)); asm("" : "+v"(v0)); asm("" : "+v"(v1));
  BAR();
  if constexpr (NEXT) {
    c.kf[0] = ldk1<0>(k0, k1); c.kf[1] = ldk1<1>(k0, k1); c.kf[2] = ldk1<2>(k0, k1); c.kf[3] = ldk1<3>(k0, k1);
    c.kf[4] = ldk1<4>(k0, k1); c.kf[5] = ldk1<5>(k0, k1); c.kf[6] = ldk1<6>(k0, k1); c.kf[7] = ldk1<7>(k0, k1);
  }
  SBAR();
  sm_slice<0>(c, scur); sm_slice<1>(c, scur); sm_slice<2>(c, scur); sm_slice<3>(c, scur);
  SBAR();
  G8(qk_gap, , NEXT>(c, snew, scur, v0, v1))
  if constexpr (!NEXT) BAR();
  G8(pv_gap, , NEXT>(c, snew))
}

extern "C" __global__ __launch_bounds__(512, 2) void gqa_block_fused(Params P) {
  extern __shared__ __attribute__((aligned(16))) char lds_[];
  const int tid = threadIdx.x, lane = tid & 63;
  const int wave = __builtin_amdgcn_readfirstlane(tid >> 6);
  const int bid = blockIdx.x;
  const int cidx = (bid & 7) * 4 + (bid >> 6), b = (bid >> 3) & 7;
  const int pi = cidx >> 4, kvh = (cidx >> 1) & 7, hh = cidx & 1;
  const int g = pi * 8 + b, si = kvh * 2 + hh;
  const u32 L0 = (u32)(size_t)lds_;
  char* ws = (char*)P.ws;
  const int h = lane >> 5, r = lane & 31, blk = (lane >> 4) & 1, q4 = (lane >> 2) & 3, p4 = lane & 3;
  const __amdgpu_buffer_rsrc_t sW = __builtin_amdgcn_make_buffer_rsrc(ws, 0, (int)WS_END, 0x00020000);
  auto ofrag = [&](u32 ks) -> u32 { const u32 dt = (ks >> 1) & 3u, wo_ = (ks >> 3) + 2u * (dt >> 1); return L0 + MS + 8192u * (wo_ ^ 2u) + 1024u * (2u * (dt & 1u) + (ks & 1u)) + 16u * lane; };
  STAMP(0);

  if (wave >= 4) {
    const int w = wave - 4;
    const size_t kvo = (size_t)(b * 8 + kvh) * 2048 * 256;
    const __amdgpu_buffer_rsrc_t sKV = __builtin_amdgcn_make_buffer_rsrc((char*)(w < 2 ? P.k[pi] : P.v[pi]) + kvo, 0, 524288, 0x00020000);
    const __amdgpu_buffer_rsrc_t sO = __builtin_amdgcn_make_buffer_rsrc((char*)P.wo[pi] + (size_t)(kvh * 4 + hh * 2) * 128 * 1024, 0, 262144, 0x00020000);
    u32 vk[2], vo[2];
#pragma unroll
    for (int i = 0; i < 2; ++i) {
      vk[i] = 256u * (u32)((lane >> 2) & 7) + 64u * (u32)h + 16u * ((u32)(lane & 3) ^ (u32)(2 * i + ((lane >> 4) & 1))) + 8192u * (u32)(w & 1);
      const u32 rm = (2 * i + h) & 3, ch = (u32)r ^ (rm << 2);
      vo[i] = (u32)h * 1024u + 256u * (ch >> 3) + 16u * (ch & 7u) + 128u;
    }
    const u32 vw = (u32)(lane >> 3) * 1024u + 16u * ((u32)(lane & 7) ^ (u32)(((lane >> 4) & 1) << 2)) + 256u * (u32)w;
    const u32 rb = L0 + (w < 2 ? KR : VR) + 8192u * (u32)(w & 1);
    u32x4 st[NREG][8];
    auto load_item = [&](int i) {
#pragma unroll
      for (int j = 0; j < 8; ++j) st[i][j] = __builtin_amdgcn_raw_buffer_load_b128(sKV, vk[(j >> 1) & 1] + 2048u * (j >> 1) + 128u * (j & 1), (u32)i * 16384u, AUX_NT);
      SBAR();
    };
    auto write_tile = [&](int i) {
#pragma unroll
      for (int j = 0; j < 8; ++j) lds_st128u(rb + (u32)(i & 3) * TB + 1024u * j + 16u * lane, st[i][j]);
      SBAR();
    };
    auto dma_tile = [&](int u) {
#pragma unroll
      for (int j = 0; j < 8; ++j) dma(rb + (u32)(u & 3) * TB + 1024u * j, vk[(j >> 1) & 1], sKV, (u32)u * 16384u + 2048u * (j >> 1) + 128u * (j & 1));
    };
    bf16x8 wr[2][16];
    const u32 aw0 = L0 + EX + 8192u * w + 128u * (4u * h + q4) + 16u * ((2u * blk + (p4 >> 1)) ^ (u32)((q4 >> 1) << 2)) + 8u * (p4 & 1);
    auto issue_w = [&](int cc) {
      const u32 c = (u32)(cc + b) & 3u;
#pragma unroll
      for (int i = 0; i < 8; ++i) dma(L0 + EX + 8192u * w + 1024u * i, vw, sO, (64u * c + 8u * i) * 1024u);
    };
    auto read_w = [&](int cc) {
#pragma unroll
      for (int kk = 0; kk < 4; ++kk)
#pragma unroll
        for (int T = 0; T < 2; ++T) { const u32 a = (aw0 ^ (64u * T)) + 2048u * kk; wr[T][4 * cc + kk] = trfrag(a, a + 1024u); }
      LGKM0();
      SBAR();
    };
    auto issue_wol = [&](int cc, u32 dst) {
      const u32 c = (u32)(cc + b) & 3u;
#pragma unroll
      for (int i = 0; i < 8; ++i) { const u32 I = 8u * w + i; dma(L0 + dst + 1024u * I, vo[i & 1], sO, (64u * c + 2u * I) * 1024u); }
    };
    BAR();
    load_item(0); load_item(1); load_item(2);
    BAR(); BAR(); BAR();
    load_item(3); load_item(4); load_item(5);
    BAR();
    write_tile(0); write_tile(1); write_tile(2); write_tile(3);
    BAR();
    auto stream = [&](auto offc) {
      constexpr int OFF = decltype(offc)::value;
#pragma unroll
      for (int t = 0; t < NT; ++t) {
        if (t - 3 + OFF >= NREG && t - 3 + OFF < NT) waitvm<16>();
        BAR();
        const int u = t + OFF;
        if (u >= 4 && u < NREG) write_tile(u);
        else if (u >= NREG && u < NT) dma_tile(u);
        if (t == 14 || t == 18 || t == 22 || t == 26) read_w((t - 14) >> 2);
        if (t == 10 || t == 14 || t == 18 || t == 22) issue_w((t - 10) >> 2);
        if (t == 27) issue_wol(3, EX);
        if (t == 29) issue_wol(0, KR);
        if (t == 30) issue_wol(1, VR);
        if (t == 31) issue_wol(2, KR + 2 * TB);
      }
    };
    if (w < 2) stream(std::integral_constant<int, 4>{}); else stream(std::integral_constant<int, 3>{});
    BAR();
    BAR();
    STAMP(5);
    DRAIN(); BAR();
    STAMP(6);
    const u64 tag = *(volatile LAS u64*)(size_t)(L0 + MISC);
    f32x16 acc[2] = {f32x16{}, f32x16{}};
    {
      bf16x8 B[16];
#pragma unroll
      for (int j = 0; j < 16; ++j) B[j] = lds128(ofrag(4u * ((u32)((j >> 2) + b) & 3u) + (j & 3)));
#pragma unroll
      for (int j = 0; j < 16; ++j) { acc[0] = MFMA(wr[0][j], B[j], acc[0]); acc[1] = MFMA(wr[1][j], B[j], acc[1]); }
    }
#pragma unroll
    for (int n = 0; n < 2; ++n)
#pragma unroll
      for (int u = 0; u < 2; ++u)
        __builtin_amdgcn_raw_buffer_store_b128(__builtin_bit_cast(u32x4, pack8(acc[n], u)), sW, 1024u * u + 16u * lane + WS_SL + (u32)((g * 16 + 4 * w + n) * 16 + si) * 2048u, 0, AUX_SC1);
    DRAIN();
    if (lane == 0) __hip_atomic_store((GAS u64*)(ws + WS_F3 + (g * 8 + wave) * 128) + si, tag, RLX_AGENT);
    STAMP(7);
    return;
  }

  const int w = wave;
  St c;
  u64 tag; u32 warm1, warm2;
  {
    const __amdgpu_buffer_rsrc_t sX = __builtin_amdgcn_make_buffer_rsrc((char*)P.x[pi] + (size_t)b * 32768, 0, 32768, 0x00020000);
    const __amdgpu_buffer_rsrc_t sQ = __builtin_amdgcn_make_buffer_rsrc((char*)P.wq[pi] + (size_t)(kvh * 512 + hh * 256) * 2, 0, 511 * 8192 + 512, 0x00020000);
    const u32 vq = (u32)(lane >> 3) * 8192u + 16u * ((u32)(lane & 7) ^ (u32)(((lane >> 4) & 1) << 2)) + 128u * (u32)w;
    auto issue_wq = [&](int cc) {
      const u32 ch = (u32)(cc + b) & 7u;
#pragma unroll
      for (int i = 0; i < 8; ++i) dma(L0 + 32768u * w + 8192u * (cc & 3) + 1024u * i, vq, sQ, (64u * ch + 8u * i) * 8192u);
    };
    {
      const GAS u64* ep = (const GAS u64*)(ws + WS_EP + g * 256);
      asm volatile("global_load_dwordx2 %0, %1, off sc1" : "=v"(tag) : "v"(ep) : "memory");
      const GAS u32* p1 = (const GAS u32*)(ws + WS_SL + (size_t)g * 16u * 16u * 2048u + (size_t)(lane & 15) * 32768u);
      const GAS u32* p2 = (const GAS u32*)((char*)P.out + (size_t)(b * 32 + (lane & 31)) * 2048);
      u32 d1, d2;
      asm volatile("global_load_dword %0, %1, off" : "=v"(d1) : "v"(p1) : "memory");
      asm volatile("global_load_dword %0, %1, off" : "=v"(d2) : "v"(p2) : "memory");
      warm1 = d1; warm2 = d2;
    }
#pragma unroll
    for (int i = 0; i < 8; ++i) { const u32 q = 8u * w + i; dma(L0 + EX + 1024u * q, (16u * lane) ^ (16u * (q & 15u)), sX, 1024u * q); }
    issue_wq(0); issue_wq(1); issue_wq(2); issue_wq(3);
    BAR();
    waitvm<32>(); BAR();
    asm volatile("" : "+v"(tag), "+v"(warm1), "+v"(warm2));
    tag += TAGC;
    STAMP(1);
    bf16x8 xf[32];
#pragma unroll
    for (int j = 0; j < 32; ++j) {
      const u32 ks = 4u * ((u32)((j >> 2) + b) & 7u) + (j & 3);
      xf[j] = lds128(L0 + EX + 1024u * r + ((32u * ks + 16u * h) ^ (16u * (r & 15))));
    }
    BAR();
    f32x16 qa[2] = {f32x16{}, f32x16{}};
    const u32 aq0 = L0 + 32768u * w + 128u * (8u * h + q4) + 16u * ((2u * blk + (p4 & 1)) ^ (u32)((q4 >> 1) << 2)) + 8u * (p4 >> 1);
#pragma unroll
    for (int cc = 0; cc < 8; ++cc) {
      if (cc <= 4) waitvm<24>(); else if (cc == 5) waitvm<16>(); else if (cc == 6) waitvm<8>(); else waitvm<0>();
      bf16x8 af[8];
#pragma unroll
      for (int kk = 0; kk < 4; ++kk)
#pragma unroll
        for (int T = 0; T < 2; ++T) { const u32 a = (aq0 ^ (64u * T)) + 8192u * (cc & 3) + 2048u * kk; af[2 * kk + T] = trfrag(a, a + 512u); }
      LGKM0();
      if (cc < 4) issue_wq(cc + 4);
      if (cc == 3) BAR();
      SBAR();
#pragma unroll
      for (int kk = 0; kk < 4; ++kk)
#pragma unroll
        for (int T = 0; T < 2; ++T) qa[T] = MFMA(af[2 * kk + T], xf[4 * cc + kk], qa[T]);
    }
    STAMP(2);
    const u32 fo = (u32)((w >> 1) * 8 + 4 * (w & 1)) * 1024u + 16u * lane;
#pragma unroll
    for (int T = 0; T < 2; ++T)
#pragma unroll
      for (int s = 0; s < 2; ++s) lds_st128(L0 + EX + fo + 1024u * (2 * T + s), pack8(qa[T], s));
  }
  const int hl = w & 1, kbw = w >> 1;
  BAR();
#pragma unroll
  for (int ks = 0; ks < 8; ++ks) c.qf[ks] = lds128(L0 + EX + (u32)(hl * 8 + ks) * 1024u + 16u * lane);
#pragma unroll
  for (int e = 0; e < 2; ++e) {
    c.ak[e] = 8192u * kbw + 2048u * (u32)(r >> 3) + 64u * (u32)(r & 7) + 16u * ((u32)(2 * e + h) ^ (u32)((r >> 2) & 3));
    c.av[e] = 8192u * kbw + 2048u * e + 64u * (u32)(4 * h + q4) + 16u * ((u32)(2 * blk + (p4 >> 1)) ^ (u32)((2 * e + h) & 3)) + 8u * (u32)(p4 & 1);
  }
#pragma unroll
  for (int d = 0; d < 4; ++d) c.o[d] = f32x16{};
  c.l = 0.f; c.bs[0] = c.bs[1] = 0.f; c.pend = false; c.al = 1.f; c.mx = 0.f;
  STAMP(3);
  BAR();
  {
    c.sA = f32x16{};
#pragma unroll
    for (int i = 0; i < 8; ++i) c.sA = MFMA(lds128(L0 + KR + c.ak[i & 1] + 512u * (i >> 1)), c.qf[i], c.sA);
    float mx = c.sA[0];
#pragma unroll
    for (int i = 1; i < 16; ++i) mx = fmaxf(mx, c.sA[i]);
    c.mc = swap_max(mx) * CSC;
  }
#pragma unroll 1
  for (int t = 0; t < NT - 2; t += 2) {
    tile_step<true>(c, c.sA, c.sB, L0, t);
    tile_step<true>(c, c.sB, c.sA, L0, t + 1);
  }
  tile_step<true>(c, c.sA, c.sB, L0, NT - 2);
  tile_step<false>(c, c.sB, c.sA, L0, NT - 1);
  const float l = swap_add(c.l), mc = c.mc;
  STAMP(4);
  auto merge = [&](auto kbc) {
    constexpr int KB = decltype(kbc)::value;
    const float inv = 1.0f / l;
    const u32 reg = L0 + MS + 8192u * w, rin = L0 + MS + 8192u * (w ^ 2);
#pragma unroll
    for (int j = 0; j < 4; ++j) {
      const f32x16& t = c.o[2 * (1 - KB) + (j >> 1)];
      u32x4 f;
#pragma unroll
      for (int e = 0; e < 4; ++e) f[e] = cvtpk(t[8 * (j & 1) + 2 * e] * inv, t[8 * (j & 1) + 2 * e + 1] * inv);
      lds_st128u(reg + 1024u * j + 16u * lane, f);
    }
    if (lane < 32) *(LAS f32x2*)(size_t)(reg + 4096u + 8u * lane) = f32x2{mc, l};
    if (w == 0 && lane == 0) *(volatile LAS u64*)(size_t)(L0 + MISC) = tag;
    BAR();
    const f32x2 st = *(const LAS f32x2*)(size_t)(rin + 4096u + 8u * r);
    u32x4 f[4];
#pragma unroll
    for (int j = 0; j < 4; ++j) f[j] = lds128u(rin + 1024u * j + 16u * lane);
    const float mn = fmaxf(mc, st[0]);
    const float a1 = __builtin_amdgcn_exp2f(mc - mn), a2 = __builtin_amdgcn_exp2f(st[0] - mn) * st[1];
    const float iv = 1.0f / (l * a1 + a2);
    const float f1 = a1 * iv, f2 = a2 * iv;
#pragma unroll
    for (int j = 0; j < 4; ++j) {
      const f32x16& t = c.o[2 * KB + (j >> 1)];
      u32x4 o4;
#pragma unroll
      for (int e = 0; e < 4; ++e)
        o4[e] = cvtpk(t[8 * (j & 1) + 2 * e] * f1 + __uint_as_float(f[j][e] << 16) * f2, t[8 * (j & 1) + 2 * e + 1] * f1 + __uint_as_float(f[j][e] & 0xffff0000u) * f2);
      lds_st128u(rin + 1024u * j + 16u * lane, o4);
    }
  };
  if (kbw) merge(std::integral_constant<int, 1>{}); else merge(std::integral_constant<int, 0>{});
  STAMP(5);
  BAR();
  STAMP(6);
  {
    f32x16 acc[2] = {f32x16{}, f32x16{}};
    const u32 ao0 = L0 + 512u * (4u * h + q4) + 16u * ((8u * w + 2u * blk + (p4 >> 1)) ^ (4u * q4)) + 8u * (p4 & 1);
#pragma unroll
    for (int cc = 0; cc < 4; ++cc) {
      bf16x8 B[4], A[8];
      const u32 buf = cc == 0 ? KR : cc == 1 ? VR : cc == 2 ? KR + 2 * TB : EX;
#pragma unroll
      for (int kk = 0; kk < 4; ++kk) {
        B[kk] = lds128(ofrag(4u * ((u32)(cc + b) & 3u) + kk));
#pragma unroll
        for (int T = 0; T < 2; ++T) { const u32 a = buf + (ao0 ^ (64u * T)) + 8192u * kk; A[2 * kk + T] = trfrag(a, a + 4096u); }
      }
#pragma unroll
      for (int kk = 0; kk < 4; ++kk) { acc[0] = MFMA(A[2 * kk], B[kk], acc[0]); acc[1] = MFMA(A[2 * kk + 1], B[kk], acc[1]); }
    }
#pragma unroll
    for (int n = 0; n < 2; ++n)
#pragma unroll
      for (int u = 0; u < 2; ++u)
        __builtin_amdgcn_raw_buffer_store_b128(__builtin_bit_cast(u32x4, pack8(acc[n], u)), sW, 1024u * u + 16u * lane + WS_SL + (u32)((g * 16 + 4 * w + 2 + n) * 16 + si) * 2048u, 0, AUX_SC1);
    DRAIN();
    if (lane == 0) __hip_atomic_store((GAS u64*)(ws + WS_F3 + (g * 8 + wave) * 128) + si, tag, RLX_AGENT);
  }
  STAMP(7);
  poll_flags((const GAS u64*)(ws + WS_F3 + (g * 8 + (si >> 2) + ((si & 2) ? 0 : 4)) * 128), 15, lane, tag);
  f32x4 sum = {0.f, 0.f, 0.f, 0.f};
  {
    u32x2 v[16];
#pragma unroll
    for (int i = 0; i < 16; ++i)
      v[i] = __builtin_amdgcn_raw_buffer_load_b64(sW, 1024u * (w >> 1) + 16u * lane + 8u * (w & 1) + 2048u * i, WS_SL + (u32)((g * 16 + si) * 16) * 2048u, AUX_SC1);
#pragma unroll
    for (int i = 0; i < 16; ++i) {
      sum[0] += __uint_as_float(v[i][0] << 16); sum[1] += __uint_as_float(v[i][0] & 0xffff0000u);
      sum[2] += __uint_as_float(v[i][1] << 16); sum[3] += __uint_as_float(v[i][1] & 0xffff0000u);
    }
  }
  u32x2 ov = {cvtpk(sum[0], sum[1]), cvtpk(sum[2], sum[3])};
  *(GAS u32x2*)((char*)P.out + ((size_t)(b * 32 + r) * 1024 + pi * 512 + 32 * si + 8 * w + 4 * h) * 2) = ov;
  if (w == 0 && lane == 0) __hip_atomic_store((GAS u64*)(ws + WS_EP + g * 256), tag, RLX_AGENT);
  STAMP(8);
}

extern "C" void kernel_launch(void* const* d_in, const int* in_sizes, int n_in, void* d_out, int out_size, void* d_ws, size_t ws_size, hipStream_t stream) {
  (void)hipFuncSetAttribute((const void*)gqa_block_fused, hipFuncAttributeMaxDynamicSharedMemorySize, (int)LDS_BYTES);
  Params P;
  for (int p = 0; p < 2; ++p) { P.x[p] = d_in[5 * p + 0]; P.k[p] = d_in[5 * p + 1]; P.v[p] = d_in[5 * p + 2]; P.wq[p] = d_in[5 * p + 3]; P.wo[p] = d_in[5 * p + 4]; }
  P.out = d_out; P.ws = d_ws;
  hipLaunchKernelGGL(gqa_block_fused, dim3(256), dim3(512), LDS_BYTES, stream, P);
}
```

```cpp
#include <hip/hip_runtime.h>
#include <stdint.h>

typedef unsigned u32;
typedef unsigned long long u64;
typedef u32 u32x4 __attribute__((ext_vector_type(4)));
typedef u32 u32x2 __attribute__((ext_vector_type(2)));
typedef float f32x2 __attribute__((ext_vector_type(2)));
typedef float f32x4 __attribute__((ext_vector_type(4)));
typedef float f32x16 __attribute__((ext_vector_type(16)));
typedef __bf16 bf16x2 __attribute__((ext_vector_type(2)));
typedef __bf16 bf16x4 __attribute__((ext_vector_type(4)));
typedef __bf16 bf16x8 __attribute__((ext_vector_type(8)));
#define LAS __attribute__((address_space(3)))
#define GAS __attribute__((address_space(1)))
#define DI __device__ __forceinline__
#define RLX_AGENT __ATOMIC_RELAXED, __HIP_MEMORY_SCOPE_AGENT
#define SBAR() __builtin_amdgcn_sched_barrier(0)
#define PIN(x) asm volatile("" : "+v"(x))

constexpr u32 RING = 0, BUFB = 32768, SCR = 131072, MISC = SCR + 16384, LDS_BYTES = 163840;
constexpr int NT = 16;
constexpr u32 WS_EP = 0;
constexpr u32 WS_FQ = 4096;
constexpr u32 WS_FO = WS_FQ + 32768;
constexpr u32 WS_F3 = WS_FO + 32768;
constexpr u32 WS_QX = 131072;
constexpr u32 OXH = 8192 + 1024;
constexpr u32 WS_OX = WS_QX + 256u * 16384u;
constexpr u32 WS_SL = WS_OX + 256u * 2u * OXH;
constexpr u32 WS_END = WS_SL + 16u * 16u * 16u * 2048u;
constexpr u32 WS_DBG = WS_END;
constexpr u64 TAGC = 0x9E3779B97F4A7C15ull;
constexpr float CSC = 0.088388347648318447f * 1.4426950408889634f;
constexpr float THR = 8.0f;
constexpr float LIM = 1024.f;
constexpr int AUX_SC1 = 16, AUX_NT = 0;
constexpr u32 SPIN_CAP = 1u << 18;

struct Params {
  const void* x[2]; const void* k[2]; const void* v[2]; const void* wq[2]; const void* wo[2];
  void* out; void* ws;
};

#define BAR() asm volatile("s_waitcnt lgkmcnt(0)\n\ts_barrier" ::: "memory")
#define BARN() asm volatile("s_waitcnt lgkmcnt(8)\n\ts_barrier" ::: "memory")
#define DRAIN() asm volatile("s_waitcnt vmcnt(0)" ::: "memory")
#define LGKM0() asm volatile("s_waitcnt lgkmcnt(0)" ::: "memory")
template <int N> DI void waitvm() { asm volatile("s_waitcnt vmcnt(%0)" ::"i"(N) : "memory"); }

DI void dma(u32 m0v, u32 voff, __amdgpu_buffer_rsrc_t srd, u32 soff) {
  asm volatile("s_mov_b32 m0, %0\n\ts_nop 0\n\tbuffer_load_dwordx4 %1, %2, %3 offen lds" ::"s"(m0v), "v"(voff), "s"(srd), "s"(soff) : "memory");
}
DI void dma_n(u32 m0v, u32 voff, __amdgpu_buffer_rsrc_t srd, u32 soff) {
  asm volatile("s_mov_b32 m0, %0\n\ts_nop 0\n\tbuffer_load_dwordx4 %1, %2, %3 offen nt lds" ::"s"(m0v), "v"(voff), "s"(srd), "s"(soff) : "memory");
}
DI u32 cvtpk(float lo, float hi) { f32x2 v = {lo, hi}; bf16x2 b = __builtin_convertvector(v, bf16x2); return __builtin_bit_cast(u32, b); }
DI bf16x8 pack8(const f32x16& x, int s) {
  u32x4 w = {cvtpk(x[8 * s], x[8 * s + 1]), cvtpk(x[8 * s + 2], x[8 * s + 3]), cvtpk(x[8 * s + 4], x[8 * s + 5]), cvtpk(x[8 * s + 6], x[8 * s + 7])};
  return __builtin_bit_cast(bf16x8, w);
}
DI bf16x8 lds128(u32 a) { return *(const LAS bf16x8*)(size_t)a; }
DI void lds_st128(u32 a, bf16x8 v) { *(LAS bf16x8*)(size_t)a = v; }
DI void lds_st128u(u32 a, u32x4 v) { *(LAS u32x4*)(size_t)a = v; }
DI bf16x8 trfrag(u32 a0, u32 a1) {
  bf16x4 lo = __builtin_amdgcn_ds_read_tr16_b64_v4bf16((LAS bf16x4*)(size_t)a0);
  bf16x4 hi = __builtin_amdgcn_ds_read_tr16_b64_v4bf16((LAS bf16x4*)(size_t)a1);
  return __builtin_shufflevector(lo, hi, 0, 1, 2, 3, 4, 5, 6, 7);
}
#define MFMA(a, b, c) __builtin_amdgcn_mfma_f32_32x32x16_bf16((a), (b), (c), 0, 0, 0)
DI float swap_max(float v) { auto r = __builtin_amdgcn_permlane32_swap(__float_as_uint(v), __float_as_uint(v), false, false); return fmaxf(__uint_as_float(r[0]), __uint_as_float(r[1])); }
DI float swap_add(float v) { auto r = __builtin_amdgcn_permlane32_swap(__float_as_uint(v), __float_as_uint(v), false, false); return __uint_as_float(r[0]) + __uint_as_float(r[1]); }

struct Chk { u32 a, b; };
DI void chk_add(Chk& c, u32 d) { c.a = __builtin_amdgcn_alignbit(c.a, c.a, 27) ^ d; c.b = __builtin_amdgcn_alignbit(c.b, c.b, 19) + d; }
DI void chk_add4(Chk& c, u32x4 v) { chk_add(c, v[0]); chk_add(c, v[1]); chk_add(c, v[2]); chk_add(c, v[3]); }
DI Chk chk_init(u64 tag) { return Chk{(u32)tag, (u32)(tag >> 32)}; }
DI u32x4 granule(u64 tag, u32 d0, u32 d1) { Chk c = chk_init(tag); chk_add(c, d0); chk_add(c, d1); chk_add(c, d0 ^ d1); return u32x4{d0, d1, c.a, c.b}; }
DI bool granule_ok(u64 tag, u32x4 v) { const u32x4 e = granule(tag, v[0], v[1]); return e[2] == v[2] && e[3] == v[3]; }
DI void poll_flags(const GAS u64* base, int mask, int lane, u64 tag) {
  const GAS u64* p = base + (lane & mask);
#pragma unroll 1
  for (u32 spins = 0; spins < SPIN_CAP; ++spins) {
    u64 v = __hip_atomic_load(p, RLX_AGENT);
    if (__all(v == tag)) break;
    __builtin_amdgcn_s_sleep(1);
  }
  asm volatile("" ::: "memory");
}
#define STAMP(i) do {} while (0)

#define PADN 0
DI void pad() { if constexpr (PADN >= 0) asm volatile("s_nop %0" :: "i"(PADN >= 0 ? PADN : 0)); }
struct St {
  bf16x8 qf[8], kf[8], vf[8];
  f32x16 o[4], sA, sB;
  u32x4 pA[2], pB[2];
  float e[2][2];
  float bs[2];
  float l, mc;
  u32 ak[2], av[2];
};
template <int G> DI void sm_slice(St& c, const f32x16& s, u32x4 (&p)[2]) {
  c.e[(G >> 1) & 1][G & 1] = __builtin_amdgcn_exp2f(fmaf(s[G], CSC, -c.mc));
  if constexpr (G >= 1) { constexpr int H = G - 1; c.bs[H & 1] += c.e[(H >> 1) & 1][H & 1]; PIN(c.bs[H & 1]); }
  if constexpr (G >= 2 && (G & 1) == 0) { constexpr int Q = (G - 2) >> 1; u32 w = cvtpk(c.e[Q & 1][0], c.e[Q & 1][1]); PIN(w); p[Q >> 2][Q & 3] = w; }
}
template <int I> DI bf16x8 ldk1(u32 a0, u32 a1, int kb) { return lds128(((I & 1) ? a1 : a0) + 8192u * kb + 512u * (I >> 1)); }
template <int I> DI bf16x8 ldv1(u32 a0, u32 a1, int kb) { return trfrag(a0 + 8192u * kb + 4096u * (I >> 2) + 512u * (I & 3), a1 + 8192u * kb + 4096u * (I >> 2) + 512u * (I & 3)); }
DI bool sm_finish(St& c, u32x4 (&p)[2]) {
  c.bs[1] += c.e[1][1];
  p[1][3] = cvtpk(c.e[1][0], c.e[1][1]);
  const float bsum = c.bs[0] + c.bs[1];
  if (__builtin_expect(__any(!(bsum <= LIM)), 0)) return false;
  c.l += bsum; c.bs[0] = 0.f; c.bs[1] = 0.f;
  return true;
}
template <int I, bool QK, bool LD, bool SM> DI void qk_gap(St& c, f32x16& snew, const f32x16& sold, u32x4 (&p)[2], u32 a0, u32 a1, int kb) {
  if constexpr (QK) { if constexpr (I == 0) snew = MFMA(c.kf[0], c.qf[0], f32x16{}); else snew = MFMA(c.kf[I], c.qf[I], snew); PIN(snew); }
  if constexpr (LD) c.kf[I] = ldk1<I>(a0, a1, kb);
  if constexpr (SM) sm_slice<8 + I>(c, sold, p);
  pad();
  SBAR();
}
template <int I, bool PV, bool LD, bool SM> DI void pv_gap(St& c, const u32x4 (&pc)[2], const f32x16& s, u32x4 (&pn)[2], u32 a0, u32 a1, int kb) {
  if constexpr (PV) { c.o[I & 3] = MFMA(c.vf[I], __builtin_bit_cast(bf16x8, pc[I >> 2]), c.o[I & 3]); PIN(c.o[I & 3]); }
  if constexpr (LD) c.vf[I] = ldv1<I>(a0, a1, kb);
  if constexpr (SM) sm_slice<I>(c, s, pn);
  pad();
  SBAR();
}
#define G8(F, ...) F<0 __VA_ARGS__; F<1 __VA_ARGS__; F<2 __VA_ARGS__; F<3 __VA_ARGS__; F<4 __VA_ARGS__; F<5 __VA_ARGS__; F<6 __VA_ARGS__; F<7 __VA_ARGS__;
DI void slow_block(St& c, u32 buf, int kb) {
  f32x16 s = f32x16{};
  const u32 a0 = buf + c.ak[0] + 8192u * kb, a1 = buf + c.ak[1] + 8192u * kb, v0 = buf + c.av[0] + 8192u * kb, v1 = buf + c.av[1] + 8192u * kb;
#pragma unroll
  for (int i = 0; i < 8; ++i) s = MFMA(lds128(((i & 1) ? a1 : a0) + 512u * (i >> 1)), c.qf[i], s);
  float mx = s[0];
#pragma unroll
  for (int i = 1; i < 16; ++i) mx = fmaxf(mx, s[i]);
  const float mn = fmaxf(c.mc, swap_max(mx) * CSC);
  const float al = __builtin_amdgcn_exp2f(c.mc - mn);
  c.mc = mn;
  float bsum = 0.f;
#pragma unroll
  for (int i = 0; i < 16; ++i) { s[i] = __builtin_amdgcn_exp2f(fmaf(s[i], CSC, -mn)); bsum += s[i]; }
  c.l = c.l * al + bsum;
#pragma unroll
  for (int d = 0; d < 4; ++d)
#pragma unroll
    for (int i = 0; i < 16; ++i) c.o[d][i] *= al;
  const bf16x8 p0 = pack8(s, 0), p1 = pack8(s, 1);
#pragma unroll
  for (int i = 0; i < 8; ++i) c.o[i & 3] = MFMA(trfrag(v0 + 4096u * (i >> 2) + 512u * (i & 3), v1 + 4096u * (i >> 2) + 512u * (i & 3)), (i >> 2) ? p1 : p0, c.o[i & 3]);
}
DI u32 ring(u32 L0, int t) { return L0 + RING + (u32)((t + 1) & 3) * BUFB; }

extern "C" __global__ __launch_bounds__(512, 2) void gqa_block_fused(Params P) {
  extern __shared__ __attribute__((aligned(16))) char lds_[];
  const int tid = threadIdx.x, lane = tid & 63;
  const int wave = __builtin_amdgcn_readfirstlane(tid >> 6);
  const int bid = blockIdx.x;
  const int cidx = (bid & 7) * 4 + (bid >> 6), b = (bid >> 3) & 7;
  const int pi = cidx >> 4, kvh = (cidx >> 1) & 7, sp = cidx & 1;
  const int g = pi * 8 + b, si = kvh * 2 + sp, wg = g * 16 + si;
  const u32 L0 = (u32)(size_t)lds_;
  char* ws = (char*)P.ws;
  const int h = lane >> 5, r = lane & 31, blk = (lane >> 4) & 1, q4 = (lane >> 2) & 3, p4 = lane & 3;
  const __amdgpu_buffer_rsrc_t sW = __builtin_amdgcn_make_buffer_rsrc(ws, 0, (int)WS_END, 0x00020000);
  STAMP(0);

  if (wave >= 4) {
    const int w = wave - 4;
    const __amdgpu_buffer_rsrc_t sX = __builtin_amdgcn_make_buffer_rsrc((char*)P.x[pi] + (size_t)b * 32768, 0, 32768, 0x00020000);
    const __amdgpu_buffer_rsrc_t sQ = __builtin_amdgcn_make_buffer_rsrc((char*)P.wq[pi] + (size_t)(kvh * 512 + sp * 256) * 2, 0, 511 * 8192 + 512, 0x00020000);
    const __amdgpu_buffer_rsrc_t sO = __builtin_amdgcn_make_buffer_rsrc((char*)P.wo[pi] + (size_t)(kvh * 4 + sp * 2) * 128 * 1024, 0, 262144, 0x00020000);
    const size_t kvo = ((size_t)(b * 8 + kvh) * 2048 + (size_t)sp * 1024) * 256;
    const __amdgpu_buffer_rsrc_t sKV = __builtin_amdgcn_make_buffer_rsrc((char*)(w < 2 ? P.k[pi] : P.v[pi]) + kvo, 0, 262144, 0x00020000);
    u32 vq[2], vo[2], vk[2];
#pragma unroll
    for (int e = 0; e < 2; ++e) {
      const u32 rm = (2 * e + h) & 3, ch = (u32)r ^ (rm << 2);
      vq[e] = (u32)h * 8192u + 16u * ch;
      vo[e] = (u32)h * 1024u + 256u * (ch >> 3) + 16u * (ch & 7u) + 128u;
    }
#pragma unroll
    for (int i = 0; i < 2; ++i) vk[i] = 256u * (u32)((lane >> 2) & 7) + 64u * (u32)h + 16u * ((u32)(lane & 3) ^ (u32)(2 * i + ((lane >> 4) & 1)));
    const u32 vw = (u32)(lane >> 3) * 1024u + 16u * ((u32)(lane & 7) ^ (u32)(((lane >> 4) & 1) << 2));

    auto issue_x = [&]() {
#pragma unroll
      for (int i = 0; i < 8; ++i) { const u32 q = 8u * w + i; dma(L0 + RING + 1024u * q, (16u * lane) ^ (16u * (q & 15u)), sX, 1024u * q); }
    };
    auto issue_wq = [&](int step) {
      const u32 c = (u32)(step - 1 + b) & 7u, buf = L0 + RING + (u32)(step & 3) * BUFB;
#pragma unroll
      for (int i = 0; i < 8; ++i) { const u32 I = 8u * w + i; dma(buf + 1024u * I, vq[i & 1], sQ, (64u * c + 2u * I) * 8192u); }
    };
    auto issue_wol = [&](int step) {
      const u32 c = (u32)(step - 25 + b) & 3u, buf = L0 + RING + (u32)(step & 3) * BUFB;
#pragma unroll
      for (int i = 0; i < 8; ++i) { const u32 I = 8u * w + i; dma(buf + 1024u * I, vo[i & 1], sO, (64u * c + 2u * I) * 1024u); }
    };
    auto issue_w = [&](int cc) {
      const u32 c = (u32)(cc + b) & 3u;
#pragma unroll
      for (int i = 0; i < 8; ++i) dma(L0 + SCR + 8192u * w + 1024u * i, vw, sO, (64u * c + 8u * i) * 1024u + 256u * w);
    };
    auto issue_kv = [&](int step) {
      const u32 t = step - 9, buf = L0 + RING + (u32)(step & 3) * BUFB + 8192u * w, so = t * 16384u + 8192u * (w & 1);
#pragma unroll
      for (int i = 0; i < 8; ++i) dma_n(buf + 1024u * i, vk[(i >> 1) & 1], sKV, so + 2048u * (i >> 1) + 128u * (i & 1));
    };
    auto issue = [&](int step) {
      if (step == 0) issue_x();
      else if (step <= 8) issue_wq(step);
      else if (step <= 24) issue_kv(step);
      else issue_wol(step);
    };
    bf16x8 wr[2][16];
    const u32 aw0 = L0 + SCR + 8192u * w + 128u * (4u * h + q4) + 16u * ((2u * blk + (p4 >> 1)) ^ (u32)((q4 >> 1) << 2)) + 8u * (p4 & 1);
    auto read_w = [&](int cc) {
#pragma unroll
      for (int kk = 0; kk < 4; ++kk)
#pragma unroll
        for (int T = 0; T < 2; ++T) { const u32 a = (aw0 ^ (64u * T)) + 2048u * kk; wr[T][4 * cc + kk] = trfrag(a, a + 1024u); }
      asm volatile("s_waitcnt lgkmcnt(0)" ::: "memory");
    };
    issue(0); issue(1); issue(2); issue(3);
    waitvm<24>(); BAR();
#pragma unroll
    for (int i = 1; i <= 8; ++i) { waitvm<16>(); BAR(); issue(i + 3); }
    BAR(); issue(12);
    waitvm<24>(); BAR(); issue_w(0);
    waitvm<24>(); BAR();
#pragma unroll
    for (int c = 0; c < 4; ++c) {
      waitvm<16>(); BAR(); issue(13 + 2 * c);
      if (c == 0) waitvm<8>(); else waitvm<16>();
      read_w(c);
      if (c < 3) issue_w(c + 1);
      if (c < 3) waitvm<16>(); else waitvm<8>();
      BAR(); issue(14 + 2 * c);
    }
    for (int i = 19; i <= 22; ++i) { waitvm<8>(); BAR(); issue_kv(i + 2); }
    waitvm<8>(); BAR(); issue(25);
    waitvm<8>(); BAR(); issue(26);
    BAR(); issue(27); issue(28);
    STAMP(5);
    DRAIN(); BAR();
    STAMP(6);
    const u64 tag = *(volatile LAS u64*)(size_t)(L0 + MISC);
    f32x16 acc[2] = {f32x16{}, f32x16{}};
    {
      bf16x8 B[16];
#pragma unroll
      for (int j = 0; j < 16; ++j) B[j] = lds128(L0 + SCR + 1024u * (4u * ((u32)((j >> 2) + b) & 3u) + (j & 3)) + 16u * lane);
#pragma unroll
      for (int j = 0; j < 16; ++j) { acc[0] = MFMA(wr[0][j], B[j], acc[0]); acc[1] = MFMA(wr[1][j], B[j], acc[1]); }
    }
#pragma unroll
    for (int n = 0; n < 2; ++n)
#pragma unroll
      for (int u = 0; u < 2; ++u)
        __builtin_amdgcn_raw_buffer_store_b128(__builtin_bit_cast(u32x4, pack8(acc[n], u)), sW, 1024u * u + 16u * lane + WS_SL + (u32)((g * 16 + 4 * w + n) * 16 + si) * 2048u, 0, AUX_SC1);
    DRAIN();
    if (lane == 0) __hip_atomic_store((GAS u64*)(ws + WS_F3 + (g * 8 + wave) * 128) + si, tag, RLX_AGENT);
    STAMP(7);
    return;
  }

  const int w = wave;
  const u64 tag = __hip_atomic_load((GAS u64*)(ws + WS_EP + g * 256), RLX_AGENT) + TAGC;
  BAR();
  STAMP(1);
  St c;
  {
    bf16x8 xf[32];
#pragma unroll
    for (int j = 0; j < 32; ++j) {
      const u32 ks = 4u * ((u32)((j >> 2) + b) & 7u) + (j & 3);
      xf[j] = lds128(L0 + RING + 1024u * r + ((32u * ks + 16u * h) ^ (16u * (r & 15))));
    }
    f32x16 qa[2] = {f32x16{}, f32x16{}};
    const u32 aq0 = 512u * (8u * h + q4) + 16u * ((8u * w + 2u * blk + (p4 & 1)) ^ (4u * q4)) + 8u * (p4 >> 1);
#pragma unroll
    for (int cc = 0; cc < 8; ++cc) {
      BAR();
      const u32 buf = L0 + RING + (u32)((cc + 1) & 3) * BUFB;
      bf16x8 af[8];
#pragma unroll
      for (int kk = 0; kk < 4; ++kk)
#pragma unroll
        for (int T = 0; T < 2; ++T) { const u32 a = buf + (aq0 ^ (64u * T)) + 8192u * kk; af[2 * kk + T] = trfrag(a, a + 2048u); }
      SBAR();
#pragma unroll
      for (int kk = 0; kk < 4; ++kk)
#pragma unroll
        for (int T = 0; T < 2; ++T) qa[T] = MFMA(af[2 * kk + T], xf[4 * cc + kk], qa[T]);
    }
    STAMP(2);
    const u32 fo = (u32)((w >> 1) * 8 + 4 * (w & 1)) * 1024u + 16u * lane;
#pragma unroll
    for (int T = 0; T < 2; ++T)
#pragma unroll
      for (int s = 0; s < 2; ++s) {
        const bf16x8 f = pack8(qa[T], s);
        lds_st128(L0 + SCR + fo + 1024u * (2 * T + s), f);
        __builtin_amdgcn_raw_buffer_store_b128(__builtin_bit_cast(u32x4, f), sW, fo + 1024u * (2 * T + s) + WS_QX + (u32)wg * 16384u, 0, AUX_SC1);
      }
  }
  const bool own = (w >> 1) == sp;
  const int hl = w & 1;
  BAR();
  if (own) {
#pragma unroll
    for (int ks = 0; ks < 8; ++ks) c.qf[ks] = lds128(L0 + SCR + (u32)(hl * 8 + ks) * 1024u + 16u * lane);
  }
  DRAIN();
  if (lane == 0) __hip_atomic_store((GAS u64*)(ws + WS_FQ + wg * 128) + w, tag, RLX_AGENT);
  if (!own) {
    poll_flags((const GAS u64*)(ws + WS_FQ + (wg ^ 1) * 128) + 2 * hl, 1, lane, tag);
#pragma unroll
    for (int ks = 0; ks < 8; ++ks)
      c.qf[ks] = __builtin_bit_cast(bf16x8, __builtin_amdgcn_raw_buffer_load_b128(sW, (u32)(hl * 8 + ks) * 1024u + 16u * lane, WS_QX + (u32)(wg ^ 1) * 16384u, AUX_SC1));
    DRAIN();
  }
  STAMP(3);
#pragma unroll
  for (int e = 0; e < 2; ++e) {
    c.ak[e] = 2048u * (u32)(r >> 3) + 64u * (u32)(r & 7) + 16u * ((u32)(2 * e + h) ^ (u32)((r >> 2) & 3));
    c.av[e] = 16384u + 2048u * e + 64u * (u32)(4 * h + q4) + 16u * ((u32)(2 * blk + (p4 >> 1)) ^ (u32)((2 * e + h) & 3)) + 8u * (u32)(p4 & 1);
  }
#pragma unroll
  for (int d = 0; d < 4; ++d) c.o[d] = f32x16{};
  c.l = 0.f; c.bs[0] = c.bs[1] = 0.f;
  BAR();
  {
    u32 a0 = ring(L0, 0) + c.ak[0], a1 = ring(L0, 0) + c.ak[1], v0 = ring(L0, 0) + c.av[0], v1 = ring(L0, 0) + c.av[1];
    asm("" : "+v"(a0)); asm("" : "+v"(a1)); asm("" : "+v"(v0)); asm("" : "+v"(v1));
#pragma unroll
    for (int i = 0; i < 8; ++i) c.kf[i] = lds128(((i & 1) ? a1 : a0) + 512u * (i >> 1));
    G8(qk_gap, , true, true, false>(c, c.sA, c.sA, c.pA, a0, a1, 1))
    float mx = c.sA[0];
#pragma unroll
    for (int i = 1; i < 16; ++i) mx = fmaxf(mx, c.sA[i]);
    c.mc = swap_max(mx) * CSC;
    G8(pv_gap, , false, true, true>(c, c.pA, c.sA, c.pA, v0, v1, 0))
  }
  int slow_t = NT, slow_kb = 0;
  {
    int t = 0;
    u32 bc = BUFB, bn = 2 * BUFB;
    for (; t < NT - 1; ++t) {
      u32 kn0 = L0 + bn + c.ak[0], kn1 = L0 + bn + c.ak[1], vc0 = L0 + bc + c.av[0], vc1 = L0 + bc + c.av[1], vn0 = L0 + bn + c.av[0], vn1 = L0 + bn + c.av[1];
      asm("" : "+v"(kn0)); asm("" : "+v"(kn1)); asm("" : "+v"(vc0)); asm("" : "+v"(vc1)); asm("" : "+v"(vn0)); asm("" : "+v"(vn1));
      BARN();
      G8(qk_gap, , true, true, true>(c, c.sB, c.sA, c.pA, kn0, kn1, 0))
      if (!sm_finish(c, c.pA)) { slow_t = t; slow_kb = 0; break; }
      G8(pv_gap, , true, true, true>(c, c.pA, c.sB, c.pB, vc0, vc1, 1))
      G8(qk_gap, , true, true, true>(c, c.sA, c.sB, c.pB, kn0, kn1, 1))
      if (!sm_finish(c, c.pB)) { slow_t = t; slow_kb = 1; break; }
      G8(pv_gap, , true, true, true>(c, c.pB, c.sA, c.pA, vn0, vn1, 0))
      bc = bn; bn = (bn + BUFB) & (4 * BUFB - 1);
    }
    if (slow_t == NT) {
      u32 vc0 = L0 + bc + c.av[0], vc1 = L0 + bc + c.av[1];
      G8(qk_gap, , true, false, true>(c, c.sB, c.sA, c.pA, 0u, 0u, 0))
      if (!sm_finish(c, c.pA)) { slow_t = NT - 1; slow_kb = 0; }
      else {
        G8(pv_gap, , true, true, true>(c, c.pA, c.sB, c.pB, vc0, vc1, 1))
        G8(qk_gap, , false, false, true>(c, c.sA, c.sB, c.pB, 0u, 0u, 0))
        if (!sm_finish(c, c.pB)) { slow_t = NT - 1; slow_kb = 1; }
        else {
          BAR();
          G8(pv_gap, , true, false, false>(c, c.pB, c.sA, c.pA, 0u, 0u, 0))
        }
      }
    }
  }
  if (__builtin_expect(slow_t < NT, 0)) {
#pragma unroll 1
    for (int t = slow_t; t < NT; ++t) {
      if (t > slow_t && t < NT - 1) BAR();
      const u32 buf = ring(L0, t);
      if (t > slow_t || slow_kb == 0) slow_block(c, buf, 0);
      slow_block(c, buf, 1);
    }
    BAR();
  }
  const float l = swap_add(c.l), mc = c.mc;
  STAMP(4);
  if (!own) {
    const u32 ob = WS_OX + (u32)(wg * 2 + hl) * OXH;
    const float inv = 1.0f / l;
    Chk ck = chk_init(tag);
#pragma unroll
    for (int d = 0; d < 4; ++d) {
#pragma unroll
      for (int i = 0; i < 16; ++i) c.o[d][i] *= inv;
#pragma unroll
      for (int s2 = 0; s2 < 2; ++s2) {
        const u32x4 f = __builtin_bit_cast(u32x4, pack8(c.o[d], s2));
        __builtin_amdgcn_raw_buffer_store_b128(f, sW, 1024u * (2 * d + s2) + 16u * lane + ob, 0, AUX_SC1);
        chk_add4(ck, f);
      }
    }
    chk_add(ck, __float_as_uint(mc)); chk_add(ck, __float_as_uint(l));
    __builtin_amdgcn_raw_buffer_store_b128(u32x4{__float_as_uint(mc), __float_as_uint(l), ck.a, ck.b}, sW, 8192u + 16u * lane + ob, 0, AUX_SC1);
    if (lane == 0) __hip_atomic_store((GAS u64*)(ws + WS_FO + wg * 128) + hl, tag, RLX_AGENT);
    if (hl == 0 && lane == 0) *(volatile LAS u64*)(size_t)(L0 + MISC) = tag;
  } else {
    const u32 ob = WS_OX + (u32)((wg ^ 1) * 2 + hl) * OXH;
    u32x4 f[8], st;
    poll_flags((const GAS u64*)(ws + WS_FO + (wg ^ 1) * 128) + hl, 0, lane, tag);
#pragma unroll 1
    for (u32 spins = 0; spins < SPIN_CAP; ++spins) {
#pragma unroll
      for (int j = 0; j < 8; ++j) f[j] = __builtin_amdgcn_raw_buffer_load_b128(sW, 1024u * j + 16u * lane, ob, AUX_SC1);
      st = __builtin_amdgcn_raw_buffer_load_b128(sW, 8192u + 16u * lane, ob, AUX_SC1);
      Chk c2 = chk_init(tag);
#pragma unroll
      for (int j = 0; j < 8; ++j) chk_add4(c2, f[j]);
      chk_add(c2, st[0]); chk_add(c2, st[1]);
      if (__all(c2.a == st[2] && c2.b == st[3])) break;
      __builtin_amdgcn_s_sleep(2);
    }
    const float m2 = __uint_as_float(st[0]), lp = __uint_as_float(st[1]);
    const float mn = fmaxf(mc, m2);
    const float a1 = __builtin_amdgcn_exp2f(mc - mn), a2 = __builtin_amdgcn_exp2f(m2 - mn) * lp;
    const float inv = 1.0f / (l * a1 + a2);
    const float f1 = a1 * inv, f2 = a2 * inv;
#pragma unroll
    for (int j = 0; j < 8; ++j) {
      u32x4 o4;
#pragma unroll
      for (int e = 0; e < 4; ++e) {
        const float lo = c.o[j >> 1][8 * (j & 1) + 2 * e] * f1 + __uint_as_float(f[j][e] << 16) * f2;
        const float hi = c.o[j >> 1][8 * (j & 1) + 2 * e + 1] * f1 + __uint_as_float(f[j][e] & 0xffff0000u) * f2;
        o4[e] = cvtpk(lo, hi);
      }
      lds_st128u(L0 + SCR + (u32)(hl * 8 + j) * 1024u + 16u * lane, o4);
    }
  }
  STAMP(5);
  BAR();
  STAMP(6);
  {
    f32x16 acc[2] = {f32x16{}, f32x16{}};
    const u32 ao0 = 512u * (4u * h + q4) + 16u * ((8u * w + 2u * blk + (p4 >> 1)) ^ (4u * q4)) + 8u * (p4 & 1);
#pragma unroll
    for (int cc = 0; cc < 4; ++cc) {
      bf16x8 B[4], A[8];
      const u32 buf = L0 + RING + (u32)((25 + cc) & 3) * BUFB;
#pragma unroll
      for (int kk = 0; kk < 4; ++kk) {
        B[kk] = lds128(L0 + SCR + 1024u * (4u * ((u32)(cc + b) & 3u) + kk) + 16u * lane);
#pragma unroll
        for (int T = 0; T < 2; ++T) { const u32 a = buf + (ao0 ^ (64u * T)) + 8192u * kk; A[2 * kk + T] = trfrag(a, a + 4096u); }
      }
#pragma unroll
      for (int kk = 0; kk < 4; ++kk) { acc[0] = MFMA(A[2 * kk], B[kk], acc[0]); acc[1] = MFMA(A[2 * kk + 1], B[kk], acc[1]); }
    }
#pragma unroll
    for (int n = 0; n < 2; ++n)
#pragma unroll
      for (int u = 0; u < 2; ++u)
        __builtin_amdgcn_raw_buffer_store_b128(__builtin_bit_cast(u32x4, pack8(acc[n], u)), sW, 1024u * u + 16u * lane + WS_SL + (u32)((g * 16 + 4 * w + 2 + n) * 16 + si) * 2048u, 0, AUX_SC1);
    DRAIN();
    if (lane == 0) __hip_atomic_store((GAS u64*)(ws + WS_F3 + (g * 8 + wave) * 128) + si, tag, RLX_AGENT);
  }
  STAMP(7);
  poll_flags((const GAS u64*)(ws + WS_F3 + (g * 8 + (si >> 2) + ((si & 2) ? 0 : 4)) * 128), 15, lane, tag);
  f32x4 sum = {0.f, 0.f, 0.f, 0.f};
  {
    u32x2 v[16];
#pragma unroll
    for (int i = 0; i < 16; ++i)
      v[i] = __builtin_amdgcn_raw_buffer_load_b64(sW, 1024u * (w >> 1) + 16u * lane + 8u * (w & 1) + 2048u * i, WS_SL + (u32)((g * 16 + si) * 16) * 2048u, AUX_SC1);
#pragma unroll
    for (int i = 0; i < 16; ++i) {
      sum[0] += __uint_as_float(v[i][0] << 16); sum[1] += __uint_as_float(v[i][0] & 0xffff0000u);
      sum[2] += __uint_as_float(v[i][1] << 16); sum[3] += __uint_as_float(v[i][1] & 0xffff0000u);
    }
  }
  u32x2 ov = {cvtpk(sum[0], sum[1]), cvtpk(sum[2], sum[3])};
  *(GAS u32x2*)((char*)P.out + ((size_t)(b * 32 + r) * 1024 + pi * 512 + 32 * si + 8 * w + 4 * h) * 2) = ov;
  if (w == 0 && lane == 0) __hip_atomic_store((GAS u64*)(ws + WS_EP + g * 256), tag, RLX_AGENT);
  STAMP(8);
}

extern "C" void kernel_launch(void* const* d_in, const int* in_sizes, int n_in, void* d_out, int out_size, void* d_ws, size_t ws_size, hipStream_t stream) {
  (void)hipFuncSetAttribute((const void*)gqa_block_fused, hipFuncAttributeMaxDynamicSharedMemorySize, (int)LDS_BYTES);
  Params P;
  for (int p = 0; p < 2; ++p) { P.x[p] = d_in[5 * p + 0]; P.k[p] = d_in[5 * p + 1]; P.v[p] = d_in[5 * p + 2]; P.wq[p] = d_in[5 * p + 3]; P.wo[p] = d_in[5 * p + 4]; }
  P.out = d_out; P.ws = d_ws;
  hipLaunchKernelGGL(gqa_block_fused, dim3(256), dim3(512), LDS_BYTES, stream, P);
}
```

```cpp
#include <hip/hip_runtime.h>
#include <stdint.h>

typedef unsigned u32;
typedef unsigned long long u64;
typedef u32 u32x4 __attribute__((ext_vector_type(4)));
typedef u32 u32x2 __attribute__((ext_vector_type(2)));
typedef float f32x2 __attribute__((ext_vector_type(2)));
typedef float f32x4 __attribute__((ext_vector_type(4)));
typedef float f32x16 __attribute__((ext_vector_type(16)));
typedef __bf16 bf16x2 __attribute__((ext_vector_type(2)));
typedef __bf16 bf16x4 __attribute__((ext_vector_type(4)));
typedef __bf16 bf16x8 __attribute__((ext_vector_type(8)));
#define LAS __attribute__((address_space(3)))
#define GAS __attribute__((address_space(1)))
#define DI __device__ __forceinline__
#define RLX_AGENT __ATOMIC_RELAXED, __HIP_MEMORY_SCOPE_AGENT
#define SBAR() __builtin_amdgcn_sched_barrier(0)
#define PIN(x) asm volatile("" : "+v"(x))

constexpr u32 RING = 0, BUFB = 32768, SCR = 131072, PST = SCR + 16384, MISC = SCR + 24576, LDS_BYTES = 163840;
#define XA 4
#define XB 4
constexpr int NY = (32 - XA - XB) / 2, NT = XA + NY + XB;
constexpr int SL = 8 + NT;
static_assert((XA + XB) % 2 == 0 && XA >= 3 && XB >= 3 && NT >= 12, "tile split");
constexpr u32 WS_EP = 0;
constexpr u32 WS_FQ = 4096;
constexpr u32 WS_FO = WS_FQ + 32768;
constexpr u32 WS_F3 = WS_FO + 32768;
constexpr u32 QXW = 4096 + 512;
constexpr u32 WS_QX = 131072;
constexpr u32 OXH = 8192 + 1024;
constexpr u32 WS_OX = WS_QX + 256u * 4u * QXW;
constexpr u32 WS_SL = WS_OX + 256u * 2u * OXH;
constexpr u32 WS_END = WS_SL + 16u * 16u * 16u * 2048u;
constexpr u32 WS_DBG = WS_END;
constexpr u64 TAGC = 0x9E3779B97F4A7C15ull;
constexpr float CSC = 0.088388347648318447f * 1.4426950408889634f;
constexpr float THR = 8.0f;
constexpr float LIM = 1024.f;
constexpr int AUX_SC1 = 16, AUX_NT = 0;
constexpr u32 SPIN_CAP = 1u << 18;

struct Params {
  const void* x[2]; const void* k[2]; const void* v[2]; const void* wq[2]; const void* wo[2];
  void* out; void* ws;
};

#define BAR() asm volatile("s_waitcnt lgkmcnt(0)\n\ts_barrier" ::: "memory")
#define BARN() asm volatile("s_waitcnt lgkmcnt(8)\n\ts_barrier" ::: "memory")
#define DRAIN() asm volatile("s_waitcnt vmcnt(0)" ::: "memory")
#define LGKM0() asm volatile("s_waitcnt lgkmcnt(0)" ::: "memory")
template <int N> DI void waitvm() { asm volatile("s_waitcnt vmcnt(%0)" ::"i"(N) : "memory"); }

DI void dma(u32 m0v, u32 voff, __amdgpu_buffer_rsrc_t srd, u32 soff) {
  asm volatile("s_mov_b32 m0, %0\n\ts_nop 0\n\tbuffer_load_dwordx4 %1, %2, %3 offen lds" ::"s"(m0v), "v"(voff), "s"(srd), "s"(soff) : "memory");
}
DI void dma_n(u32 m0v, u32 voff, __amdgpu_buffer_rsrc_t srd, u32 soff) {
  asm volatile("s_mov_b32 m0, %0\n\ts_nop 0\n\tbuffer_load_dwordx4 %1, %2, %3 offen nt lds" ::"s"(m0v), "v"(voff), "s"(srd), "s"(soff) : "memory");
}
DI u32 cvtpk(float lo, float hi) { f32x2 v = {lo, hi}; bf16x2 b = __builtin_convertvector(v, bf16x2); return __builtin_bit_cast(u32, b); }
DI bf16x8 pack8(const f32x16& x, int s) {
  u32x4 w = {cvtpk(x[8 * s], x[8 * s + 1]), cvtpk(x[8 * s + 2], x[8 * s + 3]), cvtpk(x[8 * s + 4], x[8 * s + 5]), cvtpk(x[8 * s + 6], x[8 * s + 7])};
  return __builtin_bit_cast(bf16x8, w);
}
DI bf16x8 lds128(u32 a) { return *(const LAS bf16x8*)(size_t)a; }
DI u32x4 lds128u(u32 a) { return *(const LAS u32x4*)(size_t)a; }
DI void lds_st128(u32 a, bf16x8 v) { *(LAS bf16x8*)(size_t)a = v; }
DI void lds_st128u(u32 a, u32x4 v) { *(LAS u32x4*)(size_t)a = v; }
DI bf16x8 trfrag(u32 a0, u32 a1) {
  bf16x4 lo = __builtin_amdgcn_ds_read_tr16_b64_v4bf16((LAS bf16x4*)(size_t)a0);
  bf16x4 hi = __builtin_amdgcn_ds_read_tr16_b64_v4bf16((LAS bf16x4*)(size_t)a1);
  return __builtin_shufflevector(lo, hi, 0, 1, 2, 3, 4, 5, 6, 7);
}
#define MFMA(a, b, c) __builtin_amdgcn_mfma_f32_32x32x16_bf16((a), (b), (c), 0, 0, 0)
DI float swap_max(float v) { auto r = __builtin_amdgcn_permlane32_swap(__float_as_uint(v), __float_as_uint(v), false, false); return fmaxf(__uint_as_float(r[0]), __uint_as_float(r[1])); }
DI float swap_add(float v) { auto r = __builtin_amdgcn_permlane32_swap(__float_as_uint(v), __float_as_uint(v), false, false); return __uint_as_float(r[0]) + __uint_as_float(r[1]); }

struct Chk { u32 a, b; };
DI void chk_add(Chk& c, u32 d) { c.a = __builtin_amdgcn_alignbit(c.a, c.a, 27) ^ d; c.b = __builtin_amdgcn_alignbit(c.b, c.b, 19) + d; }
DI void chk_add4(Chk& c, u32x4 v) { chk_add(c, v[0]); chk_add(c, v[1]); chk_add(c, v[2]); chk_add(c, v[3]); }
DI Chk chk_init(u64 tag) { return Chk{(u32)tag, (u32)(tag >> 32)}; }
DI u32x4 granule(u64 tag, u32 d0, u32 d1) { Chk c = chk_init(tag); chk_add(c, d0); chk_add(c, d1); chk_add(c, d0 ^ d1); return u32x4{d0, d1, c.a, c.b}; }
DI bool granule_ok(u64 tag, u32x4 v) { const u32x4 e = granule(tag, v[0], v[1]); return e[2] == v[2] && e[3] == v[3]; }
DI void poll_flags(const GAS u64* base, int mask, int lane, u64 tag) {
  const GAS u64* p = base + (lane & mask);
#pragma unroll 1
  for (u32 spins = 0; spins < SPIN_CAP; ++spins) {
    u64 v = __hip_atomic_load(p, RLX_AGENT);
    if (__all(v == tag)) break;
    __builtin_amdgcn_s_sleep(1);
  }
  asm volatile("" ::: "memory");
}
#define STAMP(i) do {} while (0)

#define PADN 0
DI void pad() { if constexpr (PADN >= 0) asm volatile("s_nop %0" :: "i"(PADN >= 0 ? PADN : 0)); }
struct St {
  bf16x8 qf[8], kf[8], vf[8];
  f32x16 o[4], sA, sB;
  u32x4 pA[2], pB[2];
  float e[2][2];
  float bs[2];
  float l, mc;
  u32 ak[2], av[2];
};
template <int G> DI void sm_slice(St& c, const f32x16& s, u32x4 (&p)[2]) {
  c.e[(G >> 1) & 1][G & 1] = __builtin_amdgcn_exp2f(fmaf(s[G], CSC, -c.mc));
  if constexpr (G >= 1) { constexpr int H = G - 1; c.bs[H & 1] += c.e[(H >> 1) & 1][H & 1]; PIN(c.bs[H & 1]); }
  if constexpr (G >= 2 && (G & 1) == 0) { constexpr int Q = (G - 2) >> 1; u32 w = cvtpk(c.e[Q & 1][0], c.e[Q & 1][1]); PIN(w); p[Q >> 2][Q & 3] = w; }
}
template <int I> DI bf16x8 ldk1(u32 a0, u32 a1, int kb) { return lds128(((I & 1) ? a1 : a0) + 8192u * kb + 512u * (I >> 1)); }
template <int I> DI bf16x8 ldv1(u32 a0, u32 a1, int kb) { return trfrag(a0 + 8192u * kb + 4096u * (I >> 2) + 512u * (I & 3), a1 + 8192u * kb + 4096u * (I >> 2) + 512u * (I & 3)); }
DI bool sm_finish(St& c, u32x4 (&p)[2]) {
  c.bs[1] += c.e[1][1];
  p[1][3] = cvtpk(c.e[1][0], c.e[1][1]);
  const float bsum = c.bs[0] + c.bs[1];
  if (__builtin_expect(__any(!(bsum <= LIM)), 0)) return false;
  c.l += bsum; c.bs[0] = 0.f; c.bs[1] = 0.f;
  return true;
}
template <int I, bool QK, bool LD, bool SM> DI void qk_gap(St& c, f32x16& snew, const f32x16& sold, u32x4 (&p)[2], u32 a0, u32 a1, int kb) {
  if constexpr (QK) { if constexpr (I == 0) snew = MFMA(c.kf[0], c.qf[0], f32x16{}); else snew = MFMA(c.kf[I], c.qf[I], snew); PIN(snew); }
  if constexpr (LD) c.kf[I] = ldk1<I>(a0, a1, kb);
  if constexpr (SM) sm_slice<8 + I>(c, sold, p);
  pad();
  SBAR();
}
template <int I, bool PV, bool LD, bool SM> DI void pv_gap(St& c, const u32x4 (&pc)[2], const f32x16& s, u32x4 (&pn)[2], u32 a0, u32 a1, int kb) {
  if constexpr (PV) { c.o[I & 3] = MFMA(c.vf[I], __builtin_bit_cast(bf16x8, pc[I >> 2]), c.o[I & 3]); PIN(c.o[I & 3]); }
  if constexpr (LD) c.vf[I] = ldv1<I>(a0, a1, kb);
  if constexpr (SM) sm_slice<I>(c, s, pn);
  pad();
  SBAR();
}
#define G8(F, ...) F<0 __VA_ARGS__; F<1 __VA_ARGS__; F<2 __VA_ARGS__; F<3 __VA_ARGS__; F<4 __VA_ARGS__; F<5 __VA_ARGS__; F<6 __VA_ARGS__; F<7 __VA_ARGS__;
DI void slow_block(St& c, u32 buf, int kb) {
  f32x16 s = f32x16{};
  const u32 a0 = buf + c.ak[0] + 8192u * kb, a1 = buf + c.ak[1] + 8192u * kb, v0 = buf + c.av[0] + 8192u * kb, v1 = buf + c.av[1] + 8192u * kb;
#pragma unroll
  for (int i = 0; i < 8; ++i) s = MFMA(lds128(((i & 1) ? a1 : a0) + 512u * (i >> 1)), c.qf[i], s);
  float mx = s[0];
#pragma unroll
  for (int i = 1; i < 16; ++i) mx = fmaxf(mx, s[i]);
  const float mn = fmaxf(c.mc, swap_max(mx) * CSC);
  const float al = __builtin_amdgcn_exp2f(c.mc - mn);
  c.mc = mn;
  float bsum = 0.f;
#pragma unroll
  for (int i = 0; i < 16; ++i) { s[i] = __builtin_amdgcn_exp2f(fmaf(s[i], CSC, -mn)); bsum += s[i]; }
  c.l = c.l * al + bsum;
#pragma unroll
  for (int d = 0; d < 4; ++d)
#pragma unroll
    for (int i = 0; i < 16; ++i) c.o[d][i] *= al;
  const bf16x8 p0 = pack8(s, 0), p1 = pack8(s, 1);
#pragma unroll
  for (int i = 0; i < 8; ++i) c.o[i & 3] = MFMA(trfrag(v0 + 4096u * (i >> 2) + 512u * (i & 3), v1 + 4096u * (i >> 2) + 512u * (i & 3)), (i >> 2) ? p1 : p0, c.o[i & 3]);
}
DI u32 ring(u32 L0, int t) { return L0 + RING + (u32)((t + 1) & 3) * BUFB; }

extern "C" __global__ __launch_bounds__(512, 2) void gqa_block_fused(Params P) {
  extern __shared__ __attribute__((aligned(16))) char lds_[];
  const int tid = threadIdx.x, lane = tid & 63;
  const int wave = __builtin_amdgcn_readfirstlane(tid >> 6);
  const int bid = blockIdx.x;
  const int cidx = (bid & 7) * 4 + (bid >> 6), b = (bid >> 3) & 7;
  const int pi = cidx >> 4, kvh = (cidx >> 1) & 7, sp = cidx & 1;
  const int g = pi * 8 + b, si = kvh * 2 + sp, wg = g * 16 + si;
  const u32 L0 = (u32)(size_t)lds_;
  char* ws = (char*)P.ws;
  const int h = lane >> 5, r = lane & 31, blk = (lane >> 4) & 1, q4 = (lane >> 2) & 3, p4 = lane & 3;
  const __amdgpu_buffer_rsrc_t sW = __builtin_amdgcn_make_buffer_rsrc(ws, 0, (int)WS_END, 0x00020000);
  STAMP(0);

  if (wave >= 4) {
    const int w = wave - 4;
    const __amdgpu_buffer_rsrc_t sX = __builtin_amdgcn_make_buffer_rsrc((char*)P.x[pi] + (size_t)b * 32768, 0, 32768, 0x00020000);
    const __amdgpu_buffer_rsrc_t sQ = __builtin_amdgcn_make_buffer_rsrc((char*)P.wq[pi] + (size_t)(kvh * 512 + sp * 256) * 2, 0, 511 * 8192 + 512, 0x00020000);
    const __amdgpu_buffer_rsrc_t sO = __builtin_amdgcn_make_buffer_rsrc((char*)P.wo[pi] + (size_t)(kvh * 4 + sp * 2) * 128 * 1024, 0, 262144, 0x00020000);
    const size_t kvo = (size_t)(b * 8 + kvh) * 2048 * 256;
    const __amdgpu_buffer_rsrc_t sKV = __builtin_amdgcn_make_buffer_rsrc((char*)(w < 2 ? P.k[pi] : P.v[pi]) + kvo, 0, 524288, 0x00020000);
    u32 vq[2], vo[2], vk[2];
#pragma unroll
    for (int e = 0; e < 2; ++e) {
      const u32 rm = (2 * e + h) & 3, ch = (u32)r ^ (rm << 2);
      vq[e] = (u32)h * 8192u + 16u * ch;
      vo[e] = (u32)h * 1024u + 256u * (ch >> 3) + 16u * (ch & 7u) + 128u;
    }
#pragma unroll
    for (int i = 0; i < 2; ++i) vk[i] = 256u * (u32)((lane >> 2) & 7) + 64u * (u32)h + 16u * ((u32)(lane & 3) ^ (u32)(2 * i + ((lane >> 4) & 1)));
    const u32 vw = (u32)(lane >> 3) * 1024u + 16u * ((u32)(lane & 7) ^ (u32)(((lane >> 4) & 1) << 2));

    auto issue_x = [&]() {
#pragma unroll
      for (int i = 0; i < 8; ++i) { const u32 q = 8u * w + i; dma(L0 + RING + 1024u * q, (16u * lane) ^ (16u * (q & 15u)), sX, 1024u * q); }
    };
    auto issue_wq = [&](int step) {
      const u32 c = (u32)(step - 1 + b) & 7u, buf = L0 + RING + (u32)(step & 3) * BUFB;
#pragma unroll
      for (int i = 0; i < 8; ++i) { const u32 I = 8u * w + i; dma(buf + 1024u * I, vq[i & 1], sQ, (64u * c + 2u * I) * 8192u); }
    };
    auto issue_wol = [&](int step) {
      const u32 c = (u32)(step - SL - 1 + b) & 3u, buf = L0 + RING + (u32)(step & 3) * BUFB;
#pragma unroll
      for (int i = 0; i < 8; ++i) { const u32 I = 8u * w + i; dma(buf + 1024u * I, vo[i & 1], sO, (64u * c + 2u * I) * 1024u); }
    };
    auto issue_w = [&](int cc) {
      const u32 c = (u32)(cc + b) & 3u;
#pragma unroll
      for (int i = 0; i < 8; ++i) dma(L0 + SCR + 8192u * w + 1024u * i, vw, sO, (64u * c + 8u * i) * 1024u + 256u * w);
    };
    auto issue_kv = [&](int step) {
      const int u = step - 9;
      const u32 gid = u < XA ? (u32)(2 * NY + u) : u < XA + NY ? (u32)(sp * NY + u - XA) : (u32)(NY + u);
      const u32 buf = L0 + RING + (u32)(step & 3) * BUFB + 8192u * w, so = gid * 16384u + 8192u * (w & 1);
#pragma unroll
      for (int i = 0; i < 8; ++i) dma_n(buf + 1024u * i, vk[(i >> 1) & 1], sKV, so + 2048u * (i >> 1) + 128u * (i & 1));
    };
    auto issue = [&](int step) {
      if (step == 0) issue_x();
      else if (step <= 8) issue_wq(step);
      else if (step <= SL) issue_kv(step);
      else issue_wol(step);
    };
    bf16x8 wr[2][16];
    const u32 aw0 = L0 + SCR + 8192u * w + 128u * (4u * h + q4) + 16u * ((2u * blk + (p4 >> 1)) ^ (u32)((q4 >> 1) << 2)) + 8u * (p4 & 1);
    auto read_w = [&](int cc) {
#pragma unroll
      for (int kk = 0; kk < 4; ++kk)
#pragma unroll
        for (int T = 0; T < 2; ++T) { const u32 a = (aw0 ^ (64u * T)) + 2048u * kk; wr[T][4 * cc + kk] = trfrag(a, a + 1024u); }
      asm volatile("s_waitcnt lgkmcnt(0)" ::: "memory");
    };
    issue(0); issue(1); issue(2); issue(3);
    waitvm<24>(); BAR();
#pragma unroll
    for (int i = 1; i <= 8; ++i) { waitvm<16>(); BAR(); issue(i + 3); }
    BAR(); issue(12);
    waitvm<24>(); BAR(); issue_w(0);
    waitvm<24>(); BAR();
#pragma unroll
    for (int c = 0; c < 4; ++c) {
      waitvm<16>(); BAR(); issue(13 + 2 * c);
      if (c == 0) waitvm<8>(); else waitvm<16>();
      read_w(c);
      if (c < 3) issue_w(c + 1);
      if (c < 3) waitvm<16>(); else waitvm<8>();
      BAR(); issue(14 + 2 * c);
    }
    for (int i = 19; i <= SL - 2; ++i) { waitvm<8>(); BAR(); issue_kv(i + 2); }
    waitvm<8>(); BAR(); issue(SL + 1);
    waitvm<8>(); BAR(); issue(SL + 2);
    BAR(); issue(SL + 3); issue(SL + 4);
    STAMP(5);
    DRAIN(); BAR();
    STAMP(6);
    const u64 tag = *(volatile LAS u64*)(size_t)(L0 + MISC);
    f32x16 acc[2] = {f32x16{}, f32x16{}};
    {
      bf16x8 B[16];
#pragma unroll
      for (int j = 0; j < 16; ++j) B[j] = lds128(L0 + SCR + 1024u * (4u * ((u32)((j >> 2) + b) & 3u) + (j & 3)) + 16u * lane);
#pragma unroll
      for (int j = 0; j < 16; ++j) { acc[0] = MFMA(wr[0][j], B[j], acc[0]); acc[1] = MFMA(wr[1][j], B[j], acc[1]); }
    }
#pragma unroll
    for (int n = 0; n < 2; ++n)
#pragma unroll
      for (int u = 0; u < 2; ++u)
        __builtin_amdgcn_raw_buffer_store_b128(__builtin_bit_cast(u32x4, pack8(acc[n], u)), sW, 1024u * u + 16u * lane + WS_SL + (u32)((g * 16 + 4 * w + n) * 16 + si) * 2048u, 0, AUX_SC1);
    DRAIN();
    if (lane == 0) __hip_atomic_store((GAS u64*)(ws + WS_F3 + (g * 8 + wave) * 128) + si, tag, RLX_AGENT);
    STAMP(7);
    return;
  }

  const int w = wave;
  const u64 tag = __hip_atomic_load((GAS u64*)(ws + WS_EP + g * 256), RLX_AGENT) + TAGC;
  BAR();
  STAMP(1);
  St c;
  {
    bf16x8 xf[32];
#pragma unroll
    for (int j = 0; j < 32; ++j) {
      const u32 ks = 4u * ((u32)((j >> 2) + b) & 7u) + (j & 3);
      xf[j] = lds128(L0 + RING + 1024u * r + ((32u * ks + 16u * h) ^ (16u * (r & 15))));
    }
    f32x16 qa[2] = {f32x16{}, f32x16{}};
    const u32 aq0 = 512u * (8u * h + q4) + 16u * ((8u * w + 2u * blk + (p4 & 1)) ^ (4u * q4)) + 8u * (p4 >> 1);
#pragma unroll
    for (int cc = 0; cc < 8; ++cc) {
      BAR();
      const u32 buf = L0 + RING + (u32)((cc + 1) & 3) * BUFB;
      bf16x8 af[8];
#pragma unroll
      for (int kk = 0; kk < 4; ++kk)
#pragma unroll
        for (int T = 0; T < 2; ++T) { const u32 a = buf + (aq0 ^ (64u * T)) + 8192u * kk; af[2 * kk + T] = trfrag(a, a + 2048u); }
      SBAR();
#pragma unroll
      for (int kk = 0; kk < 4; ++kk)
#pragma unroll
        for (int T = 0; T < 2; ++T) qa[T] = MFMA(af[2 * kk + T], xf[4 * cc + kk], qa[T]);
    }
    STAMP(2);
    const u32 fo = (u32)((w >> 1) * 8 + 4 * (w & 1)) * 1024u + 16u * lane;
    Chk ck = chk_init(tag);
#pragma unroll
    for (int T = 0; T < 2; ++T)
#pragma unroll
      for (int s = 0; s < 2; ++s) {
        const bf16x8 f = pack8(qa[T], s);
        lds_st128(L0 + SCR + fo + 1024u * (2 * T + s), f);
        __builtin_amdgcn_raw_buffer_store_b128(__builtin_bit_cast(u32x4, f), sW, 1024u * (2 * T + s) + 16u * lane + WS_QX + (u32)(wg * 4 + w) * QXW, 0, AUX_SC1);
        chk_add4(ck, __builtin_bit_cast(u32x4, f));
      }
    __builtin_amdgcn_raw_buffer_store_b64(u32x2{ck.a, ck.b}, sW, 4096u + 8u * lane + WS_QX + (u32)(wg * 4 + w) * QXW, 0, AUX_SC1);
  }
  const bool own = (w >> 1) == sp;
  const int hl = w & 1;
  BAR();
  if (own) {
#pragma unroll
    for (int ks = 0; ks < 8; ++ks) c.qf[ks] = lds128(L0 + SCR + (u32)(hl * 8 + ks) * 1024u + 16u * lane);
  } else {
#pragma unroll
    for (int k = 0; k <= XA - 2; ++k) BAR();
    u32x4 f[8]; u32x2 cv[2];
    auto fetch = [&]() {
#pragma unroll
      for (int pw = 0; pw < 2; ++pw) {
        const u32 so = WS_QX + (u32)((wg ^ 1) * 4 + 2 * hl + pw) * QXW;
#pragma unroll
        for (int j = 0; j < 4; ++j) f[4 * pw + j] = __builtin_amdgcn_raw_buffer_load_b128(sW, 1024u * j + 16u * lane, so, AUX_SC1);
        cv[pw] = __builtin_amdgcn_raw_buffer_load_b64(sW, 4096u + 8u * lane, so, AUX_SC1);
      }
    };
    fetch();
    BAR();
#pragma unroll 1
    for (u32 spins = 0; spins < SPIN_CAP; ++spins) {
      bool ok = true;
#pragma unroll
      for (int pw = 0; pw < 2; ++pw) {
        Chk c2 = chk_init(tag);
#pragma unroll
        for (int j = 0; j < 4; ++j) chk_add4(c2, f[4 * pw + j]);
        ok = ok && c2.a == cv[pw][0] && c2.b == cv[pw][1];
      }
      if (__all(ok)) break;
      __builtin_amdgcn_s_sleep(1);
      fetch();
    }
#pragma unroll
    for (int ks = 0; ks < 8; ++ks) c.qf[ks] = __builtin_bit_cast(bf16x8, f[ks]);
  }
  STAMP(3);
#pragma unroll
  for (int e = 0; e < 2; ++e) {
    c.ak[e] = 2048u * (u32)(r >> 3) + 64u * (u32)(r & 7) + 16u * ((u32)(2 * e + h) ^ (u32)((r >> 2) & 3));
    c.av[e] = 16384u + 2048u * e + 64u * (u32)(4 * h + q4) + 16u * ((u32)(2 * blk + (p4 >> 1)) ^ (u32)((2 * e + h) & 3)) + 8u * (u32)(p4 & 1);
  }
#pragma unroll
  for (int d = 0; d < 4; ++d) c.o[d] = f32x16{};
  c.l = 0.f; c.bs[0] = c.bs[1] = 0.f;
  const int t0 = own ? 0 : XA, t1 = own ? NT : XA + NY;
  BAR();
  {
    u32 a0 = ring(L0, t0) + c.ak[0], a1 = ring(L0, t0) + c.ak[1], v0 = ring(L0, t0) + c.av[0], v1 = ring(L0, t0) + c.av[1];
    asm("" : "+v"(a0)); asm("" : "+v"(a1)); asm("" : "+v"(v0)); asm("" : "+v"(v1));
#pragma unroll
    for (int i = 0; i < 8; ++i) c.kf[i] = lds128(((i & 1) ? a1 : a0) + 512u * (i >> 1));
    G8(qk_gap, , true, true, false>(c, c.sA, c.sA, c.pA, a0, a1, 1))
    float mx = c.sA[0];
#pragma unroll
    for (int i = 1; i < 16; ++i) mx = fmaxf(mx, c.sA[i]);
    c.mc = swap_max(mx) * CSC;
    G8(pv_gap, , false, true, true>(c, c.pA, c.sA, c.pA, v0, v1, 0))
  }
  int slow_t = NT, slow_kb = 0;
  {
    int t = t0;
    u32 bc = (u32)((t0 + 1) & 3) * BUFB, bn = (u32)((t0 + 2) & 3) * BUFB;
    for (; t < t1 - 1; ++t) {
      u32 kn0 = L0 + bn + c.ak[0], kn1 = L0 + bn + c.ak[1], vc0 = L0 + bc + c.av[0], vc1 = L0 + bc + c.av[1], vn0 = L0 + bn + c.av[0], vn1 = L0 + bn + c.av[1];
      asm("" : "+v"(kn0)); asm("" : "+v"(kn1)); asm("" : "+v"(vc0)); asm("" : "+v"(vc1)); asm("" : "+v"(vn0)); asm("" : "+v"(vn1));
      BARN();
      G8(qk_gap, , true, true, true>(c, c.sB, c.sA, c.pA, kn0, kn1, 0))
      if (!sm_finish(c, c.pA)) { slow_t = t; slow_kb = 0; break; }
      G8(pv_gap, , true, true, true>(c, c.pA, c.sB, c.pB, vc0, vc1, 1))
      G8(qk_gap, , true, true, true>(c, c.sA, c.sB, c.pB, kn0, kn1, 1))
      if (!sm_finish(c, c.pB)) { slow_t = t; slow_kb = 1; break; }
      G8(pv_gap, , true, true, true>(c, c.pB, c.sA, c.pA, vn0, vn1, 0))
      bc = bn; bn = (bn + BUFB) & (4 * BUFB - 1);
    }
    if (slow_t == NT) {
      u32 vc0 = L0 + bc + c.av[0], vc1 = L0 + bc + c.av[1];
      if (!own) BAR();
      G8(qk_gap, , true, false, true>(c, c.sB, c.sA, c.pA, 0u, 0u, 0))
      if (!sm_finish(c, c.pA)) { slow_t = t1 - 1; slow_kb = 0; }
      else {
        G8(pv_gap, , true, true, true>(c, c.pA, c.sB, c.pB, vc0, vc1, 1))
        G8(qk_gap, , false, false, true>(c, c.sA, c.sB, c.pB, 0u, 0u, 0))
        if (!sm_finish(c, c.pB)) { slow_t = t1 - 1; slow_kb = 1; }
        else {
          if (own) BAR();
          G8(pv_gap, , true, false, false>(c, c.pB, c.sA, c.pA, 0u, 0u, 0))
        }
      }
    }
  }
  if (__builtin_expect(slow_t < NT, 0)) {
#pragma unroll 1
    for (int t = slow_t; t < t1; ++t) {
      if (t > slow_t && t < NT - 1) BAR();
      const u32 buf = ring(L0, t);
      if (t > slow_t || slow_kb == 0) slow_block(c, buf, 0);
      slow_block(c, buf, 1);
    }
    if (own) BAR();
  }
  const float l = swap_add(c.l), mc = c.mc;
  STAMP(4);
  if (!own) {
    {
      const u32 ob = WS_OX + (u32)(wg * 2 + hl) * OXH;
      const float inv = 1.0f / l;
      Chk ck = chk_init(tag);
#pragma unroll
      for (int d = 0; d < 4; ++d) {
#pragma unroll
        for (int i = 0; i < 16; ++i) c.o[d][i] *= inv;
#pragma unroll
        for (int s2 = 0; s2 < 2; ++s2) {
          const u32x4 f = __builtin_bit_cast(u32x4, pack8(c.o[d], s2));
          __builtin_amdgcn_raw_buffer_store_b128(f, sW, 1024u * (2 * d + s2) + 16u * lane + ob, 0, AUX_SC1);
          chk_add4(ck, f);
        }
      }
      chk_add(ck, __float_as_uint(mc)); chk_add(ck, __float_as_uint(l));
      __builtin_amdgcn_raw_buffer_store_b128(u32x4{__float_as_uint(mc), __float_as_uint(l), ck.a, ck.b}, sW, 8192u + 16u * lane + ob, 0, AUX_SC1);
    }
#pragma unroll
    for (int k = 0; k < XB - 2; ++k) BAR();
    const u32 ob = WS_OX + (u32)((wg ^ 1) * 2 + hl) * OXH;
    u32x4 f[8], st;
    auto fetch = [&]() {
#pragma unroll
      for (int j = 0; j < 8; ++j) f[j] = __builtin_amdgcn_raw_buffer_load_b128(sW, 1024u * j + 16u * lane, ob, AUX_SC1);
      st = __builtin_amdgcn_raw_buffer_load_b128(sW, 8192u + 16u * lane, ob, AUX_SC1);
    };
    fetch();
    BAR();
#pragma unroll 1
    for (u32 spins = 0; spins < SPIN_CAP; ++spins) {
      Chk c2 = chk_init(tag);
#pragma unroll
      for (int j = 0; j < 8; ++j) chk_add4(c2, f[j]);
      chk_add(c2, st[0]); chk_add(c2, st[1]);
      if (__all(c2.a == st[2] && c2.b == st[3])) break;
      __builtin_amdgcn_s_sleep(1);
      fetch();
    }
#pragma unroll
    for (int j = 0; j < 8; ++j) lds_st128u(L0 + SCR + (u32)(hl * 8 + j) * 1024u + 16u * lane, f[j]);
    if (lane < 32) *(LAS u32x2*)(size_t)(L0 + PST + 256u * hl + 8u * lane) = u32x2{st[0], st[1]};
    if (hl == 0 && lane == 0) *(volatile LAS u64*)(size_t)(L0 + MISC) = tag;
    BAR();
  } else {
    const u32x2 st = *(const LAS u32x2*)(size_t)(L0 + PST + 256u * hl + 8u * r);
    u32x4 f[8];
#pragma unroll
    for (int j = 0; j < 8; ++j) f[j] = lds128u(L0 + SCR + (u32)(hl * 8 + j) * 1024u + 16u * lane);
    const float m2 = __uint_as_float(st[0]), lp = __uint_as_float(st[1]);
    const float mn = fmaxf(mc, m2);
    const float a1 = __builtin_amdgcn_exp2f(mc - mn), a2 = __builtin_amdgcn_exp2f(m2 - mn) * lp;
    const float inv = 1.0f / (l * a1 + a2);
    const float f1 = a1 * inv, f2 = a2 * inv;
#pragma unroll
    for (int j = 0; j < 8; ++j) {
      u32x4 o4;
#pragma unroll
      for (int e = 0; e < 4; ++e) {
        const float lo = c.o[j >> 1][8 * (j & 1) + 2 * e] * f1 + __uint_as_float(f[j][e] << 16) * f2;
        const float hi = c.o[j >> 1][8 * (j & 1) + 2 * e + 1] * f1 + __uint_as_float(f[j][e] & 0xffff0000u) * f2;
        o4[e] = cvtpk(lo, hi);
      }
      lds_st128u(L0 + SCR + (u32)(hl * 8 + j) * 1024u + 16u * lane, o4);
    }
  }
  STAMP(5);
  BAR();
  STAMP(6);
  {
    f32x16 acc[2] = {f32x16{}, f32x16{}};
    const u32 ao0 = 512u * (4u * h + q4) + 16u * ((8u * w + 2u * blk + (p4 >> 1)) ^ (4u * q4)) + 8u * (p4 & 1);
#pragma unroll
    for (int cc = 0; cc < 4; ++cc) {
      bf16x8 B[4], A[8];
      const u32 buf = L0 + RING + (u32)((SL + 1 + cc) & 3) * BUFB;
#pragma unroll
      for (int kk = 0; kk < 4; ++kk) {
        B[kk] = lds128(L0 + SCR + 1024u * (4u * ((u32)(cc + b) & 3u) + kk) + 16u * lane);
#pragma unroll
        for (int T = 0; T < 2; ++T) { const u32 a = buf + (ao0 ^ (64u * T)) + 8192u * kk; A[2 * kk + T] = trfrag(a, a + 4096u); }
      }
#pragma unroll
      for (int kk = 0; kk < 4; ++kk) { acc[0] = MFMA(A[2 * kk], B[kk], acc[0]); acc[1] = MFMA(A[2 * kk + 1], B[kk], acc[1]); }
    }
#pragma unroll
    for (int n = 0; n < 2; ++n)
#pragma unroll
      for (int u = 0; u < 2; ++u)
        __builtin_amdgcn_raw_buffer_store_b128(__builtin_bit_cast(u32x4, pack8(acc[n], u)), sW, 1024u * u + 16u * lane + WS_SL + (u32)((g * 16 + 4 * w + 2 + n) * 16 + si) * 2048u, 0, AUX_SC1);
    DRAIN();
    if (lane == 0) __hip_atomic_store((GAS u64*)(ws + WS_F3 + (g * 8 + wave) * 128) + si, tag, RLX_AGENT);
  }
  STAMP(7);
  poll_flags((const GAS u64*)(ws + WS_F3 + (g * 8 + (si >> 2) + ((si & 2) ? 0 : 4)) * 128), 15, lane, tag);
  f32x4 sum = {0.f, 0.f, 0.f, 0.f};
  {
    u32x2 v[16];
#pragma unroll
    for (int i = 0; i < 16; ++i)
      v[i] = __builtin_amdgcn_raw_buffer_load_b64(sW, 1024u * (w >> 1) + 16u * lane + 8u * (w & 1) + 2048u * i, WS_SL + (u32)((g * 16 + si) * 16) * 2048u, AUX_SC1);
#pragma unroll
    for (int i = 0; i < 16; ++i) {
      sum[0] += __uint_as_float(v[i][0] << 16); sum[1] += __uint_as_float(v[i][0] & 0xffff0000u);
      sum[2] += __uint_as_float(v[i][1] << 16); sum[3] += __uint_as_float(v[i][1] & 0xffff0000u);
    }
  }
  u32x2 ov = {cvtpk(sum[0], sum[1]), cvtpk(sum[2], sum[3])};
  *(GAS u32x2*)((char*)P.out + ((size_t)(b * 32 + r) * 1024 + pi * 512 + 32 * si + 8 * w + 4 * h) * 2) = ov;
  if (w == 0 && lane == 0) __hip_atomic_store((GAS u64*)(ws + WS_EP + g * 256), tag, RLX_AGENT);
  STAMP(8);
}

extern "C" void kernel_launch(void* const* d_in, const int* in_sizes, int n_in, void* d_out, int out_size, void* d_ws, size_t ws_size, hipStream_t stream) {
  (void)hipFuncSetAttribute((const void*)gqa_block_fused, hipFuncAttributeMaxDynamicSharedMemorySize, (int)LDS_BYTES);
  Params P;
  for (int p = 0; p < 2; ++p) { P.x[p] = d_in[5 * p + 0]; P.k[p] = d_in[5 * p + 1]; P.v[p] = d_in[5 * p + 2]; P.wq[p] = d_in[5 * p + 3]; P.wo[p] = d_in[5 * p + 4]; }
  P.out = d_out; P.ws = d_ws;
  hipLaunchKernelGGL(gqa_block_fused, dim3(256), dim3(512), LDS_BYTES, stream, P);
}
```

```cpp
#include <hip/hip_runtime.h>
#include <stdint.h>

typedef unsigned u32;
typedef unsigned long long u64;
typedef u32 u32x4 __attribute__((ext_vector_type(4)));
typedef u32 u32x2 __attribute__((ext_vector_type(2)));
typedef float f32x2 __attribute__((ext_vector_type(2)));
typedef float f32x4 __attribute__((ext_vector_type(4)));
typedef float f32x16 __attribute__((ext_vector_type(16)));
typedef __bf16 bf16x2 __attribute__((ext_vector_type(2)));
typedef __bf16 bf16x4 __attribute__((ext_vector_type(4)));
typedef __bf16 bf16x8 __attribute__((ext_vector_type(8)));
#define LAS __attribute__((address_space(3)))
#define GAS __attribute__((address_space(1)))
#define DI __device__ __forceinline__
#define RLX_AGENT __ATOMIC_RELAXED, __HIP_MEMORY_SCOPE_AGENT
#define SBAR() __builtin_amdgcn_sched_barrier(0)
#define PIN(x) asm volatile("" : "+v"(x))

constexpr u32 RING = 0, BUFB = 32768, KR = 0, VR = 65536, TB = 16384, SCR = 131072, PST = SCR + 16384, MISC = SCR + 24576, LDS_BYTES = 163840;
#define XA 4
#define XB 4
constexpr int NY = (32 - XA - XB) / 2, NT = XA + NY + XB;
constexpr int SL = 8 + NT;
static_assert(XA == 4 && XB == 4 && NT == 20, "the end-of-stream LDS map below is written for 20 tiles");
constexpr u32 WS_EP = 0;
constexpr u32 WS_FQ = 4096;
constexpr u32 WS_FO = WS_FQ + 32768;
constexpr u32 WS_F3 = WS_FO + 32768;
constexpr u32 QXW = 4096 + 512;
constexpr u32 WS_QX = 131072;
constexpr u32 OXH = 8192 + 1024;
constexpr u32 WS_OX = WS_QX + 256u * 4u * QXW;
constexpr u32 WS_SL = WS_OX + 256u * 2u * OXH;
constexpr u32 WS_END = WS_SL + 16u * 16u * 16u * 2048u;
constexpr u32 WS_DBG = WS_END;
constexpr u64 TAGC = 0x9E3779B97F4A7C15ull;
constexpr float CSC = 0.088388347648318447f * 1.4426950408889634f;
constexpr float THR = 8.0f;
constexpr float LIM = 1024.f;
constexpr int AUX_SC1 = 16, AUX_NT = 0;
constexpr u32 SPIN_CAP = 1u << 18;

struct Params {
  const void* x[2]; const void* k[2]; const void* v[2]; const void* wq[2]; const void* wo[2];
  void* out; void* ws;
};

#define BAR() asm volatile("s_waitcnt lgkmcnt(0)\n\ts_barrier" ::: "memory")
#define BARN() asm volatile("s_waitcnt lgkmcnt(8)\n\ts_barrier" ::: "memory")
#define DRAIN() asm volatile("s_waitcnt vmcnt(0)" ::: "memory")
#define LGKM0() asm volatile("s_waitcnt lgkmcnt(0)" ::: "memory")
template <int N> DI void waitvm() { asm volatile("s_waitcnt vmcnt(%0)" ::"i"(N) : "memory"); }

DI void dma(u32 m0v, u32 voff, __amdgpu_buffer_rsrc_t srd, u32 soff) {
  asm volatile("s_mov_b32 m0, %0\n\ts_nop 0\n\tbuffer_load_dwordx4 %1, %2, %3 offen lds" ::"s"(m0v), "v"(voff), "s"(srd), "s"(soff) : "memory");
}
DI void dma_n(u32 m0v, u32 voff, __amdgpu_buffer_rsrc_t srd, u32 soff) {
  asm volatile("s_mov_b32 m0, %0\n\ts_nop 0\n\tbuffer_load_dwordx4 %1, %2, %3 offen nt lds" ::"s"(m0v), "v"(voff), "s"(srd), "s"(soff) : "memory");
}
DI u32 cvtpk(float lo, float hi) { f32x2 v = {lo, hi}; bf16x2 b = __builtin_convertvector(v, bf16x2); return __builtin_bit_cast(u32, b); }
DI bf16x8 pack8(const f32x16& x, int s) {
  u32x4 w = {cvtpk(x[8 * s], x[8 * s + 1]), cvtpk(x[8 * s + 2], x[8 * s + 3]), cvtpk(x[8 * s + 4], x[8 * s + 5]), cvtpk(x[8 * s + 6], x[8 * s + 7])};
  return __builtin_bit_cast(bf16x8, w);
}
DI bf16x8 lds128(u32 a) { return *(const LAS bf16x8*)(size_t)a; }
DI u32x4 lds128u(u32 a) { return *(const LAS u32x4*)(size_t)a; }
DI void lds_st128(u32 a, bf16x8 v) { *(LAS bf16x8*)(size_t)a = v; }
DI void lds_st128u(u32 a, u32x4 v) { *(LAS u32x4*)(size_t)a = v; }
DI bf16x8 trfrag(u32 a0, u32 a1) {
  bf16x4 lo = __builtin_amdgcn_ds_read_tr16_b64_v4bf16((LAS bf16x4*)(size_t)a0);
  bf16x4 hi = __builtin_amdgcn_ds_read_tr16_b64_v4bf16((LAS bf16x4*)(size_t)a1);
  return __builtin_shufflevector(lo, hi, 0, 1, 2, 3, 4, 5, 6, 7);
}
#define MFMA(a, b, c) __builtin_amdgcn_mfma_f32_32x32x16_bf16((a), (b), (c), 0, 0, 0)
DI float swap_max(float v) { auto r = __builtin_amdgcn_permlane32_swap(__float_as_uint(v), __float_as_uint(v), false, false); return fmaxf(__uint_as_float(r[0]), __uint_as_float(r[1])); }
DI float swap_add(float v) { auto r = __builtin_amdgcn_permlane32_swap(__float_as_uint(v), __float_as_uint(v), false, false); return __uint_as_float(r[0]) + __uint_as_float(r[1]); }

struct Chk { u32 a, b; };
DI void chk_add(Chk& c, u32 d) { c.a = __builtin_amdgcn_alignbit(c.a, c.a, 27) ^ d; c.b = __builtin_amdgcn_alignbit(c.b, c.b, 19) + d; }
DI void chk_add4(Chk& c, u32x4 v) { chk_add(c, v[0]); chk_add(c, v[1]); chk_add(c, v[2]); chk_add(c, v[3]); }
DI Chk chk_init(u64 tag) { return Chk{(u32)tag, (u32)(tag >> 32)}; }
DI u32x4 granule(u64 tag, u32 d0, u32 d1) { Chk c = chk_init(tag); chk_add(c, d0); chk_add(c, d1); chk_add(c, d0 ^ d1); return u32x4{d0, d1, c.a, c.b}; }
DI bool granule_ok(u64 tag, u32x4 v) { const u32x4 e = granule(tag, v[0], v[1]); return e[2] == v[2] && e[3] == v[3]; }
DI void poll_flags(const GAS u64* base, int mask, int lane, u64 tag) {
  const GAS u64* p = base + (lane & mask);
#pragma unroll 1
  for (u32 spins = 0; spins < SPIN_CAP; ++spins) {
    u64 v = __hip_atomic_load(p, RLX_AGENT);
    if (__all(v == tag)) break;
    __builtin_amdgcn_s_sleep(1);
  }
  asm volatile("" ::: "memory");
}
#define STAMP(i) do {} while (0)

#define PADN 0
DI void pad() { if constexpr (PADN >= 0) asm volatile("s_nop %0" :: "i"(PADN >= 0 ? PADN : 0)); }
struct St {
  bf16x8 qf[8], kf[8], vf[8];
  f32x16 o[4], sA, sB;
  u32x4 pA[2], pB[2];
  float e[2][2];
  float bs[2];
  float l, mc;
  u32 ak[2], av[2];
};
template <int G> DI void sm_slice(St& c, const f32x16& s, u32x4 (&p)[2]) {
  c.e[(G >> 1) & 1][G & 1] = __builtin_amdgcn_exp2f(fmaf(s[G], CSC, -c.mc));
  if constexpr (G >= 1) { constexpr int H = G - 1; c.bs[H & 1] += c.e[(H >> 1) & 1][H & 1]; PIN(c.bs[H & 1]); }
  if constexpr (G >= 2 && (G & 1) == 0) { constexpr int Q = (G - 2) >> 1; u32 w = cvtpk(c.e[Q & 1][0], c.e[Q & 1][1]); PIN(w); p[Q >> 2][Q & 3] = w; }
}
template <int I> DI bf16x8 ldk1(u32 a0, u32 a1, int kb) { return lds128(((I & 1) ? a1 : a0) + 8192u * kb + 512u * (I >> 1)); }
template <int I> DI bf16x8 ldv1(u32 a0, u32 a1, int kb) { return trfrag(a0 + 8192u * kb + 4096u * (I >> 2) + 512u * (I & 3), a1 + 8192u * kb + 4096u * (I >> 2) + 512u * (I & 3)); }
DI bool sm_finish(St& c, u32x4 (&p)[2]) {
  c.bs[1] += c.e[1][1];
  p[1][3] = cvtpk(c.e[1][0], c.e[1][1]);
  const float bsum = c.bs[0] + c.bs[1];
  if (__builtin_expect(__any(!(bsum <= LIM)), 0)) return false;
  c.l += bsum; c.bs[0] = 0.f; c.bs[1] = 0.f;
  return true;
}
template <int I, bool QK, int LD, bool SM> DI void qk_gap(St& c, f32x16& snew, const f32x16& sold, u32x4 (&p)[2], u32 a0, u32 a1, int kb) {
  if constexpr (QK) { if constexpr (I == 0) snew = MFMA(c.kf[0], c.qf[0], f32x16{}); else snew = MFMA(c.kf[I], c.qf[I], snew); PIN(snew); }
  if constexpr (LD == 1) c.kf[I] = ldk1<I>(a0, a1, kb);
  if constexpr (LD == 2) c.vf[I] = ldv1<I>(a0, a1, kb);
  if constexpr (SM) sm_slice<8 + I>(c, sold, p);
  pad();
  SBAR();
}
template <int I, bool PV, bool LD, bool SM> DI void pv_gap(St& c, const u32x4 (&pc)[2], const f32x16& s, u32x4 (&pn)[2], u32 a0, u32 a1, int kb) {
  if constexpr (PV) { c.o[I & 3] = MFMA(c.vf[I], __builtin_bit_cast(bf16x8, pc[I >> 2]), c.o[I & 3]); PIN(c.o[I & 3]); }
  if constexpr (LD) c.kf[I] = ldk1<I>(a0, a1, kb);
  if constexpr (SM) sm_slice<I>(c, s, pn);
  pad();
  SBAR();
}
#define G8(F, ...) F<0 __VA_ARGS__; F<1 __VA_ARGS__; F<2 __VA_ARGS__; F<3 __VA_ARGS__; F<4 __VA_ARGS__; F<5 __VA_ARGS__; F<6 __VA_ARGS__; F<7 __VA_ARGS__;
DI void slow_block(St& c, __amdgpu_buffer_rsrc_t sK, u32 koff  , u32 vbuf, int kb) {
  f32x16 s = f32x16{};
  const u32 v0 = vbuf + c.av[0] + 8192u * kb, v1 = vbuf + c.av[1] + 8192u * kb;
#pragma unroll
  for (int i = 0; i < 8; ++i) s = MFMA(__builtin_bit_cast(bf16x8, __builtin_amdgcn_raw_buffer_load_b128(sK, koff + 8192u * kb + 32u * i, 0, 0)), c.qf[i], s);
  float mx = s[0];
#pragma unroll
  for (int i = 1; i < 16; ++i) mx = fmaxf(mx, s[i]);
  const float mn = fmaxf(c.mc, swap_max(mx) * CSC);
  const float al = __builtin_amdgcn_exp2f(c.mc - mn);
  c.mc = mn;
  float bsum = 0.f;
#pragma unroll
  for (int i = 0; i < 16; ++i) { s[i] = __builtin_amdgcn_exp2f(fmaf(s[i], CSC, -mn)); bsum += s[i]; }
  c.l = c.l * al + bsum;
#pragma unroll
  for (int d = 0; d < 4; ++d)
#pragma unroll
    for (int i = 0; i < 16; ++i) c.o[d][i] *= al;
  const bf16x8 p0 = pack8(s, 0), p1 = pack8(s, 1);
#pragma unroll
  for (int i = 0; i < 8; ++i) c.o[i & 3] = MFMA(trfrag(v0 + 4096u * (i >> 2) + 512u * (i & 3), v1 + 4096u * (i >> 2) + 512u * (i & 3)), (i >> 2) ? p1 : p0, c.o[i & 3]);
}
DI u32 kbuf(u32 L0, int t) { return L0 + KR + (u32)((t + 2) & 3) * TB; }
DI u32 vbuf(u32 L0, int t) { return L0 + VR + (u32)(t & 3) * TB; }
DI u32 tile_id(int u, int sp) { return u < XA ? (u32)(2 * NY + u) : u < XA + NY ? (u32)(sp * NY + u - XA) : (u32)(NY + u); }

extern "C" __global__ __launch_bounds__(512, 2) void gqa_block_fused(Params P) {
  extern __shared__ __attribute__((aligned(16))) char lds_[];
  const int tid = threadIdx.x, lane = tid & 63;
  const int wave = __builtin_amdgcn_readfirstlane(tid >> 6);
  const int bid = blockIdx.x;
  const int cidx = (bid & 7) * 4 + (bid >> 6), b = (bid >> 3) & 7;
  const int pi = cidx >> 4, kvh = (cidx >> 1) & 7, sp = cidx & 1;
  const int g = pi * 8 + b, si = kvh * 2 + sp, wg = g * 16 + si;
  const u32 L0 = (u32)(size_t)lds_;
  char* ws = (char*)P.ws;
  const int h = lane >> 5, r = lane & 31, blk = (lane >> 4) & 1, q4 = (lane >> 2) & 3, p4 = lane & 3;
  const __amdgpu_buffer_rsrc_t sW = __builtin_amdgcn_make_buffer_rsrc(ws, 0, (int)WS_END, 0x00020000);
  STAMP(0);

  if (wave >= 4) {
    const int w = wave - 4;
    const __amdgpu_buffer_rsrc_t sX = __builtin_amdgcn_make_buffer_rsrc((char*)P.x[pi] + (size_t)b * 32768, 0, 32768, 0x00020000);
    const __amdgpu_buffer_rsrc_t sQ = __builtin_amdgcn_make_buffer_rsrc((char*)P.wq[pi] + (size_t)(kvh * 512 + sp * 256) * 2, 0, 511 * 8192 + 512, 0x00020000);
    const __amdgpu_buffer_rsrc_t sO = __builtin_amdgcn_make_buffer_rsrc((char*)P.wo[pi] + (size_t)(kvh * 4 + sp * 2) * 128 * 1024, 0, 262144, 0x00020000);
    const size_t kvo = (size_t)(b * 8 + kvh) * 2048 * 256;
    const __amdgpu_buffer_rsrc_t sKV = __builtin_amdgcn_make_buffer_rsrc((char*)(w < 2 ? P.k[pi] : P.v[pi]) + kvo, 0, 524288, 0x00020000);
    u32 vq[2], vo[2], vk[2];
#pragma unroll
    for (int e = 0; e < 2; ++e) {
      const u32 rm = (2 * e + h) & 3, ch = (u32)r ^ (rm << 2);
      vq[e] = (u32)h * 8192u + 16u * ch;
      vo[e] = (u32)h * 1024u + 256u * (ch >> 3) + 16u * (ch & 7u) + 128u;
    }
#pragma unroll
    for (int i = 0; i < 2; ++i) vk[i] = 256u * (u32)((lane >> 2) & 7) + 64u * (u32)h + 16u * ((u32)(lane & 3) ^ (u32)(2 * i + ((lane >> 4) & 1)));
    const u32 vw = (u32)(lane >> 3) * 1024u + 16u * ((u32)(lane & 7) ^ (u32)(((lane >> 4) & 1) << 2));

    auto issue_x = [&]() {
#pragma unroll
      for (int i = 0; i < 8; ++i) { const u32 q = 8u * w + i; dma(L0 + RING + 1024u * q, (16u * lane) ^ (16u * (q & 15u)), sX, 1024u * q); }
    };
    auto issue_wq = [&](int step) {
      const u32 c = (u32)(step - 1 + b) & 7u, buf = L0 + RING + (u32)(step & 3) * BUFB;
#pragma unroll
      for (int i = 0; i < 8; ++i) { const u32 I = 8u * w + i; dma(buf + 1024u * I, vq[i & 1], sQ, (64u * c + 2u * I) * 8192u); }
    };
    auto issue_wol = [&](int cc, u32 dst) {
      const u32 c = (u32)(cc + b) & 3u, buf = L0 + dst;
#pragma unroll
      for (int i = 0; i < 8; ++i) { const u32 I = 8u * w + i; dma(buf + 1024u * I, vo[i & 1], sO, (64u * c + 2u * I) * 1024u); }
    };
    auto issue_w = [&](int cc) {
      const u32 c = (u32)(cc + b) & 3u;
#pragma unroll
      for (int i = 0; i < 8; ++i) dma(L0 + SCR + 8192u * w + 1024u * i, vw, sO, (64u * c + 8u * i) * 1024u + 256u * w);
    };
    const int isv = w >> 1;
    auto issue_kv = [&](int u) {
      const u32 buf = (isv ? vbuf(L0, u) : kbuf(L0, u)) + 8192u * (w & 1), so = tile_id(u, sp) * 16384u + 8192u * (w & 1);
#pragma unroll
      for (int i = 0; i < 8; ++i) dma_n(buf + 1024u * i, vk[(i >> 1) & 1], sKV, so + 2048u * (i >> 1) + 128u * (i & 1));
    };
    auto issue = [&](int step) { if (step == 0) issue_x(); else issue_wq(step); };
    bf16x8 wr[2][16];
    const u32 aw0 = L0 + SCR + 8192u * w + 128u * (4u * h + q4) + 16u * ((2u * blk + (p4 >> 1)) ^ (u32)((q4 >> 1) << 2)) + 8u * (p4 & 1);
    auto read_w = [&](int cc) {
#pragma unroll
      for (int kk = 0; kk < 4; ++kk)
#pragma unroll
        for (int T = 0; T < 2; ++T) { const u32 a = (aw0 ^ (64u * T)) + 2048u * kk; wr[T][4 * cc + kk] = trfrag(a, a + 1024u); }
      asm volatile("s_waitcnt lgkmcnt(0)" ::: "memory");
    };
    issue(0); issue(1); issue(2); issue(3);
    waitvm<24>(); BAR();
#pragma unroll
    for (int i = 1; i <= 5; ++i) { waitvm<16>(); BAR(); issue(i + 3); }
    waitvm<16>(); BAR(); if (!isv) { issue_kv(0); issue_kv(1); }
    if (isv) waitvm<8>(); else waitvm<24>();
    BAR(); if (isv) { issue_kv(0); issue_kv(1); }
    if (isv) waitvm<16>(); else waitvm<16>();
    BAR(); if (isv) { issue_kv(2); issue_kv(3); }
    BAR(); if (!isv) { issue_kv(2); issue_kv(3); }
    waitvm<24>(); BAR(); issue_w(0);
#pragma unroll
    for (int i = 1; i <= 9; ++i) {
      if (i == 3 || i == 5 || i == 7) waitvm<32>(); else waitvm<24>();
      BAR();
      if (i + 3 - isv >= 4) issue_kv(i + 3 - isv);
      if ((i & 1) == 0) { if (i == 2 && isv) waitvm<8>(); else waitvm<16>(); read_w((i >> 1) - 1); if (i < 8) issue_w(i >> 1); }
    }
    for (int i = 10; i <= NT - 3; ++i) { waitvm<16>(); BAR(); if (i + 3 - isv < NT) issue_kv(i + 3 - isv); }
    waitvm<8>(); BAR(); issue_wol(0, RING + 1 * BUFB);
    waitvm<8>(); BAR(); issue_wol(1, RING + 2 * BUFB);
    waitvm<16>(); BAR(); issue_wol(2, RING + 0 * BUFB);
    BAR(); issue_wol(3, RING + 3 * BUFB);
    STAMP(5);
    DRAIN(); BAR();
    STAMP(6);
    const u64 tag = *(volatile LAS u64*)(size_t)(L0 + MISC);
    f32x16 acc[2] = {f32x16{}, f32x16{}};
    {
      bf16x8 B[16];
#pragma unroll
      for (int j = 0; j < 16; ++j) B[j] = lds128(L0 + SCR + 1024u * (4u * ((u32)((j >> 2) + b) & 3u) + (j & 3)) + 16u * lane);
#pragma unroll
      for (int j = 0; j < 16; ++j) { acc[0] = MFMA(wr[0][j], B[j], acc[0]); acc[1] = MFMA(wr[1][j], B[j], acc[1]); }
    }
#pragma unroll
    for (int n = 0; n < 2; ++n)
#pragma unroll
      for (int u = 0; u < 2; ++u)
        __builtin_amdgcn_raw_buffer_store_b128(__builtin_bit_cast(u32x4, pack8(acc[n], u)), sW, 1024u * u + 16u * lane + WS_SL + (u32)((g * 16 + 4 * w + n) * 16 + si) * 2048u, 0, AUX_SC1);
    DRAIN();
    if (lane == 0) __hip_atomic_store((GAS u64*)(ws + WS_F3 + (g * 8 + wave) * 128) + si, tag, RLX_AGENT);
    STAMP(7);
    return;
  }

  const int w = wave;
  const u64 tag = __hip_atomic_load((GAS u64*)(ws + WS_EP + g * 256), RLX_AGENT) + TAGC;
  BAR();
  STAMP(1);
  St c;
  {
    bf16x8 xf[32];
#pragma unroll
    for (int j = 0; j < 32; ++j) {
      const u32 ks = 4u * ((u32)((j >> 2) + b) & 7u) + (j & 3);
      xf[j] = lds128(L0 + RING + 1024u * r + ((32u * ks + 16u * h) ^ (16u * (r & 15))));
    }
    f32x16 qa[2] = {f32x16{}, f32x16{}};
    const u32 aq0 = 512u * (8u * h + q4) + 16u * ((8u * w + 2u * blk + (p4 & 1)) ^ (4u * q4)) + 8u * (p4 >> 1);
#pragma unroll
    for (int cc = 0; cc < 8; ++cc) {
      BAR();
      const u32 buf = L0 + RING + (u32)((cc + 1) & 3) * BUFB;
      bf16x8 af[8];
#pragma unroll
      for (int kk = 0; kk < 4; ++kk)
#pragma unroll
        for (int T = 0; T < 2; ++T) { const u32 a = buf + (aq0 ^ (64u * T)) + 8192u * kk; af[2 * kk + T] = trfrag(a, a + 2048u); }
      SBAR();
#pragma unroll
      for (int kk = 0; kk < 4; ++kk)
#pragma unroll
        for (int T = 0; T < 2; ++T) qa[T] = MFMA(af[2 * kk + T], xf[4 * cc + kk], qa[T]);
    }
    STAMP(2);
    const u32 fo = (u32)((w >> 1) * 8 + 4 * (w & 1)) * 1024u + 16u * lane;
    Chk ck = chk_init(tag);
#pragma unroll
    for (int T = 0; T < 2; ++T)
#pragma unroll
      for (int s = 0; s < 2; ++s) {
        const bf16x8 f = pack8(qa[T], s);
        lds_st128(L0 + SCR + fo + 1024u * (2 * T + s), f);
        __builtin_amdgcn_raw_buffer_store_b128(__builtin_bit_cast(u32x4, f), sW, 1024u * (2 * T + s) + 16u * lane + WS_QX + (u32)(wg * 4 + w) * QXW, 0, AUX_SC1);
        chk_add4(ck, __builtin_bit_cast(u32x4, f));
      }
    __builtin_amdgcn_raw_buffer_store_b64(u32x2{ck.a, ck.b}, sW, 4096u + 8u * lane + WS_QX + (u32)(wg * 4 + w) * QXW, 0, AUX_SC1);
  }
  const bool own = (w >> 1) == sp;
  const int hl = w & 1;
  BAR();
  if (own) {
#pragma unroll
    for (int ks = 0; ks < 8; ++ks) c.qf[ks] = lds128(L0 + SCR + (u32)(hl * 8 + ks) * 1024u + 16u * lane);
  } else {
#pragma unroll
    for (int k = 0; k <= XA - 2; ++k) BAR();
    u32x4 f[8]; u32x2 cv[2];
    auto fetch = [&]() {
#pragma unroll
      for (int pw = 0; pw < 2; ++pw) {
        const u32 so = WS_QX + (u32)((wg ^ 1) * 4 + 2 * hl + pw) * QXW;
#pragma unroll
        for (int j = 0; j < 4; ++j) f[4 * pw + j] = __builtin_amdgcn_raw_buffer_load_b128(sW, 1024u * j + 16u * lane, so, AUX_SC1);
        cv[pw] = __builtin_amdgcn_raw_buffer_load_b64(sW, 4096u + 8u * lane, so, AUX_SC1);
      }
    };
    fetch();
    BAR();
#pragma unroll 1
    for (u32 spins = 0; spins < SPIN_CAP; ++spins) {
      bool ok = true;
#pragma unroll
      for (int pw = 0; pw < 2; ++pw) {
        Chk c2 = chk_init(tag);
#pragma unroll
        for (int j = 0; j < 4; ++j) chk_add4(c2, f[4 * pw + j]);
        ok = ok && c2.a == cv[pw][0] && c2.b == cv[pw][1];
      }
      if (__all(ok)) break;
      __builtin_amdgcn_s_sleep(1);
      fetch();
    }
#pragma unroll
    for (int ks = 0; ks < 8; ++ks) c.qf[ks] = __builtin_bit_cast(bf16x8, f[ks]);
  }
  STAMP(3);
#pragma unroll
  for (int e = 0; e < 2; ++e) {
    c.ak[e] = 2048u * (u32)(r >> 3) + 64u * (u32)(r & 7) + 16u * ((u32)(2 * e + h) ^ (u32)((r >> 2) & 3));
    c.av[e] = 2048u * e + 64u * (u32)(4 * h + q4) + 16u * ((u32)(2 * blk + (p4 >> 1)) ^ (u32)((2 * e + h) & 3)) + 8u * (u32)(p4 & 1);
  }
#pragma unroll
  for (int d = 0; d < 4; ++d) c.o[d] = f32x16{};
  c.l = 0.f; c.bs[0] = c.bs[1] = 0.f;
  const int t0 = own ? 0 : XA, t1 = own ? NT : XA + NY;
  BAR();
  c.mc = 0.f;
#pragma unroll 1
  for (int t = t0;;) {
    {
      u32 a0 = kbuf(L0, t) + c.ak[0], a1 = kbuf(L0, t) + c.ak[1];
      asm("" : "+v"(a0)); asm("" : "+v"(a1));
#pragma unroll
      for (int i = 0; i < 8; ++i) c.kf[i] = lds128(((i & 1) ? a1 : a0) + 512u * (i >> 1));
      G8(qk_gap, , true, 1, false>(c, c.sA, c.sA, c.pA, a0, a1, 1))
      if (t == t0) {
        float mx = c.sA[0];
#pragma unroll
        for (int i = 1; i < 16; ++i) mx = fmaxf(mx, c.sA[i]);
        c.mc = swap_max(mx) * CSC;
      }
      c.bs[0] = 0.f; c.bs[1] = 0.f;
      G8(pv_gap, , false, false, true>(c, c.pA, c.sA, c.pA, 0u, 0u, 0))
    }
    int fail = -1;
    for (; t < t1 - 1; ++t) {
      u32 kn0 = kbuf(L0, t + 1) + c.ak[0], kn1 = kbuf(L0, t + 1) + c.ak[1], vc0 = vbuf(L0, t) + c.av[0], vc1 = vbuf(L0, t) + c.av[1];
      asm("" : "+v"(kn0)); asm("" : "+v"(kn1)); asm("" : "+v"(vc0)); asm("" : "+v"(vc1));
      BAR();
      G8(qk_gap, , true, 2, true>(c, c.sB, c.sA, c.pA, vc0, vc1, 0))
      if (!sm_finish(c, c.pA)) { fail = 0; break; }
      G8(pv_gap, , true, true, true>(c, c.pA, c.sB, c.pB, kn0, kn1, 0))
      G8(qk_gap, , true, 2, true>(c, c.sA, c.sB, c.pB, vc0, vc1, 1))
      if (!sm_finish(c, c.pB)) { fail = 1; break; }
      G8(pv_gap, , true, true, true>(c, c.pB, c.sA, c.pA, kn0, kn1, 1))
    }
    if (fail < 0) {
      u32 vc0 = vbuf(L0, t) + c.av[0], vc1 = vbuf(L0, t) + c.av[1];
      BAR();
      G8(qk_gap, , true, 2, true>(c, c.sB, c.sA, c.pA, vc0, vc1, 0))
      if (!sm_finish(c, c.pA)) fail = 0;
      else {
        G8(pv_gap, , true, false, true>(c, c.pA, c.sB, c.pB, 0u, 0u, 0))
        G8(qk_gap, , false, 2, true>(c, c.sA, c.sB, c.pB, vc0, vc1, 1))
        if (!sm_finish(c, c.pB)) fail = 1;
        else {
          if (own) BAR();
          G8(pv_gap, , true, false, false>(c, c.pB, c.sA, c.pA, 0u, 0u, 0))
          break;
        }
      }
    }
    {
      const __amdgpu_buffer_rsrc_t sK = __builtin_amdgcn_make_buffer_rsrc((char*)P.k[pi] + (size_t)(b * 8 + kvh) * 2048 * 256, 0, 524288, 0x00020000);
      const u32 koff = tile_id(t, sp) * 16384u + 256u * (u32)r + 16u * (u32)h;
      if (fail == 0) slow_block(c, sK, koff, vbuf(L0, t), 0);
      slow_block(c, sK, koff, vbuf(L0, t), 1);
    }
    if (++t == t1) { if (own) BAR(); break; }
  }
  const float l = swap_add(c.l), mc = c.mc;
  STAMP(4);
  if (!own) {
    {
      const u32 ob = WS_OX + (u32)(wg * 2 + hl) * OXH;
      const float inv = 1.0f / l;
      Chk ck = chk_init(tag);
#pragma unroll
      for (int d = 0; d < 4; ++d) {
#pragma unroll
        for (int i = 0; i < 16; ++i) c.o[d][i] *= inv;
#pragma unroll
        for (int s2 = 0; s2 < 2; ++s2) {
          const u32x4 f = __builtin_bit_cast(u32x4, pack8(c.o[d], s2));
          __builtin_amdgcn_raw_buffer_store_b128(f, sW, 1024u * (2 * d + s2) + 16u * lane + ob, 0, AUX_SC1);
          chk_add4(ck, f);
        }
      }
      chk_add(ck, __float_as_uint(mc)); chk_add(ck, __float_as_uint(l));
      __builtin_amdgcn_raw_buffer_store_b128(u32x4{__float_as_uint(mc), __float_as_uint(l), ck.a, ck.b}, sW, 8192u + 16u * lane + ob, 0, AUX_SC1);
    }
#pragma unroll
    for (int k = 0; k < XB - 1; ++k) BAR();
    const u32 ob = WS_OX + (u32)((wg ^ 1) * 2 + hl) * OXH;
    u32x4 f[8], st;
    auto fetch = [&]() {
#pragma unroll
      for (int j = 0; j < 8; ++j) f[j] = __builtin_amdgcn_raw_buffer_load_b128(sW, 1024u * j + 16u * lane, ob, AUX_SC1);
      st = __builtin_amdgcn_raw_buffer_load_b128(sW, 8192u + 16u * lane, ob, AUX_SC1);
    };
    fetch();
    BAR();
#pragma unroll 1
    for (u32 spins = 0; spins < SPIN_CAP; ++spins) {
      Chk c2 = chk_init(tag);
#pragma unroll
      for (int j = 0; j < 8; ++j) chk_add4(c2, f[j]);
      chk_add(c2, st[0]); chk_add(c2, st[1]);
      if (__all(c2.a == st[2] && c2.b == st[3])) break;
      __builtin_amdgcn_s_sleep(1);
      fetch();
    }
#pragma unroll
    for (int j = 0; j < 8; ++j) lds_st128u(L0 + SCR + (u32)(hl * 8 + j) * 1024u + 16u * lane, f[j]);
    if (lane < 32) *(LAS u32x2*)(size_t)(L0 + PST + 256u * hl + 8u * lane) = u32x2{st[0], st[1]};
    if (hl == 0 && lane == 0) *(volatile LAS u64*)(size_t)(L0 + MISC) = tag;
    BAR();
  } else {
    const u32x2 st = *(const LAS u32x2*)(size_t)(L0 + PST + 256u * hl + 8u * r);
    u32x4 f[8];
#pragma unroll
    for (int j = 0; j < 8; ++j) f[j] = lds128u(L0 + SCR + (u32)(hl * 8 + j) * 1024u + 16u * lane);
    const float m2 = __uint_as_float(st[0]), lp = __uint_as_float(st[1]);
    const float mn = fmaxf(mc, m2);
    const float a1 = __builtin_amdgcn_exp2f(mc - mn), a2 = __builtin_amdgcn_exp2f(m2 - mn) * lp;
    const float inv = 1.0f / (l * a1 + a2);
    const float f1 = a1 * inv, f2 = a2 * inv;
#pragma unroll
    for (int j = 0; j < 8; ++j) {
      u32x4 o4;
#pragma unroll
      for (int e = 0; e < 4; ++e) {
        const float lo = c.o[j >> 1][8 * (j & 1) + 2 * e] * f1 + __uint_as_float(f[j][e] << 16) * f2;
        const float hi = c.o[j >> 1][8 * (j & 1) + 2 * e + 1] * f1 + __uint_as_float(f[j][e] & 0xffff0000u) * f2;
        o4[e] = cvtpk(lo, hi);
      }
      lds_st128u(L0 + SCR + (u32)(hl * 8 + j) * 1024u + 16u * lane, o4);
    }
  }
  STAMP(5);
  BAR();
  STAMP(6);
  {
    f32x16 acc[2] = {f32x16{}, f32x16{}};
    const u32 ao0 = 512u * (4u * h + q4) + 16u * ((8u * w + 2u * blk + (p4 >> 1)) ^ (4u * q4)) + 8u * (p4 & 1);
#pragma unroll
    for (int cc = 0; cc < 4; ++cc) {
      bf16x8 B[4], A[8];
      const u32 buf = L0 + RING + (cc == 0 ? 1u : cc == 1 ? 2u : cc == 2 ? 0u : 3u) * BUFB;
#pragma unroll
      for (int kk = 0; kk < 4; ++kk) {
        B[kk] = lds128(L0 + SCR + 1024u * (4u * ((u32)(cc + b) & 3u) + kk) + 16u * lane);
#pragma unroll
        for (int T = 0; T < 2; ++T) { const u32 a = buf + (ao0 ^ (64u * T)) + 8192u * kk; A[2 * kk + T] = trfrag(a, a + 4096u); }
      }
#pragma unroll
      for (int kk = 0; kk < 4; ++kk) { acc[0] = MFMA(A[2 * kk], B[kk], acc[0]); acc[1] = MFMA(A[2 * kk + 1], B[kk], acc[1]); }
    }
#pragma unroll
    for (int n = 0; n < 2; ++n)
#pragma unroll
      for (int u = 0; u < 2; ++u)
        __builtin_amdgcn_raw_buffer_store_b128(__builtin_bit_cast(u32x4, pack8(acc[n], u)), sW, 1024u * u + 16u * lane + WS_SL + (u32)((g * 16 + 4 * w + 2 + n) * 16 + si) * 2048u, 0, AUX_SC1);
    DRAIN();
    if (lane == 0) __hip_atomic_store((GAS u64*)(ws + WS_F3 + (g * 8 + wave) * 128) + si, tag, RLX_AGENT);
  }
  STAMP(7);
  poll_flags((const GAS u64*)(ws + WS_F3 + (g * 8 + (si >> 2) + ((si & 2) ? 0 : 4)) * 128), 15, lane, tag);
  f32x4 sum = {0.f, 0.f, 0.f, 0.f};
  {
    u32x2 v[16];
#pragma unroll
    for (int i = 0; i < 16; ++i)
      v[i] = __builtin_amdgcn_raw_buffer_load_b64(sW, 1024u * (w >> 1) + 16u * lane + 8u * (w & 1) + 2048u * i, WS_SL + (u32)((g * 16 + si) * 16) * 2048u, AUX_SC1);
#pragma unroll
    for (int i = 0; i < 16; ++i) {
      sum[0] += __uint_as_float(v[i][0] << 16); sum[1] += __uint_as_float(v[i][0] & 0xffff0000u);
      sum[2] += __uint_as_float(v[i][1] << 16); sum[3] += __uint_as_float(v[i][1] & 0xffff0000u);
    }
  }
  u32x2 ov = {cvtpk(sum[0], sum[1]), cvtpk(sum[2], sum[3])};
  *(GAS u32x2*)((char*)P.out + ((size_t)(b * 32 + r) * 1024 + pi * 512 + 32 * si + 8 * w + 4 * h) * 2) = ov;
  if (w == 0 && lane == 0) __hip_atomic_store((GAS u64*)(ws + WS_EP + g * 256), tag, RLX_AGENT);
  STAMP(8);
}

extern "C" void kernel_launch(void* const* d_in, const int* in_sizes, int n_in, void* d_out, int out_size, void* d_ws, size_t ws_size, hipStream_t stream) {
  (void)hipFuncSetAttribute((const void*)gqa_block_fused, hipFuncAttributeMaxDynamicSharedMemorySize, (int)LDS_BYTES);
  Params P;
  for (int p = 0; p < 2; ++p) { P.x[p] = d_in[5 * p + 0]; P.k[p] = d_in[5 * p + 1]; P.v[p] = d_in[5 * p + 2]; P.wq[p] = d_in[5 * p + 3]; P.wo[p] = d_in[5 * p + 4]; }
  P.out = d_out; P.ws = d_ws;
  hipLaunchKernelGGL(gqa_block_fused, dim3(256), dim3(512), LDS_BYTES, stream, P);
}
```

```cpp
#include <hip/hip_runtime.h>
#include <stdint.h>

typedef unsigned u32;
typedef unsigned long long u64;
typedef u32 u32x4 __attribute__((ext_vector_type(4)));
typedef u32 u32x2 __attribute__((ext_vector_type(2)));
typedef float f32x2 __attribute__((ext_vector_type(2)));
typedef float f32x4 __attribute__((ext_vector_type(4)));
typedef float f32x16 __attribute__((ext_vector_type(16)));
typedef __bf16 bf16x2 __attribute__((ext_vector_type(2)));
typedef __bf16 bf16x4 __attribute__((ext_vector_type(4)));
typedef __bf16 bf16x8 __attribute__((ext_vector_type(8)));
#define LAS __attribute__((address_space(3)))
#define GAS __attribute__((address_space(1)))
#define DI __device__ __forceinline__
#define RLX_AGENT __ATOMIC_RELAXED, __HIP_MEMORY_SCOPE_AGENT
#define SBAR() __builtin_amdgcn_sched_barrier(0)
#define PIN(x) asm volatile("" : "+v"(x))

constexpr u32 RING = 0, BUFB = 32768, KR = 0, VR = 65536, TB = 16384, SCR = 131072, PST = SCR + 16384, MISC = SCR + 24576, LDS_BYTES = 163840;
constexpr int XA = 4, XB = 4;
constexpr int NY = (32 - XA - XB) / 2, NT = XA + NY + XB;
static_assert(XA == 4 && XB == 4 && NT == 20, "the end-of-stream LDS map below is written for 20 tiles");
constexpr u32 WS_EP = 0;
constexpr u32 WS_F3 = 4096;
constexpr u32 QXW = 4096 + 512;
constexpr u32 WS_QX = 131072;
constexpr u32 OXH = 8192 + 1024;
constexpr u32 WS_OX = WS_QX + 256u * 4u * QXW;
constexpr u32 WS_SL = WS_OX + 256u * 2u * OXH;
constexpr u32 WS_END = WS_SL + 16u * 16u * 16u * 2048u;
constexpr u64 TAGC = 0x9E3779B97F4A7C15ull;
constexpr float CSC = 0.088388347648318447f * 1.4426950408889634f;
constexpr float THR = 8.0f;
constexpr int AUX_SC1 = 16, AUX_NT = 0;
constexpr u32 SPIN_CAP = 1u << 18;

struct Params {
  const void* x[2]; const void* k[2]; const void* v[2]; const void* wq[2]; const void* wo[2];
  void* out; void* ws;
};

#define BAR() asm volatile("s_waitcnt lgkmcnt(0)\n\ts_barrier" ::: "memory")
#define BARN() asm volatile("s_waitcnt lgkmcnt(8)\n\ts_barrier" ::: "memory")
#define DRAIN() asm volatile("s_waitcnt vmcnt(0)" ::: "memory")
#define LGKM0() asm volatile("s_waitcnt lgkmcnt(0)" ::: "memory")
template <int N> DI void waitvm() { asm volatile("s_waitcnt vmcnt(%0)" ::"i"(N) : "memory"); }

DI void dma(u32 m0v, u32 voff, __amdgpu_buffer_rsrc_t srd, u32 soff) {
  asm volatile("s_mov_b32 m0, %0\n\ts_nop 0\n\tbuffer_load_dwordx4 %1, %2, %3 offen lds" ::"s"(m0v), "v"(voff), "s"(srd), "s"(soff) : "memory");
}
DI void dma_n(u32 m0v, u32 voff, __amdgpu_buffer_rsrc_t srd, u32 soff) {
  asm volatile("s_mov_b32 m0, %0\n\ts_nop 0\n\tbuffer_load_dwordx4 %1, %2, %3 offen nt lds" ::"s"(m0v), "v"(voff), "s"(srd), "s"(soff) : "memory");
}
DI u32 cvtpk(float lo, float hi) { f32x2 v = {lo, hi}; bf16x2 b = __builtin_convertvector(v, bf16x2); return __builtin_bit_cast(u32, b); }
DI bf16x8 pack8(const f32x16& x, int s) {
  u32x4 w = {cvtpk(x[8 * s], x[8 * s + 1]), cvtpk(x[8 * s + 2], x[8 * s + 3]), cvtpk(x[8 * s + 4], x[8 * s + 5]), cvtpk(x[8 * s + 6], x[8 * s + 7])};
  return __builtin_bit_cast(bf16x8, w);
}
DI bf16x8 lds128(u32 a) { return *(const LAS bf16x8*)(size_t)a; }
DI u32x4 lds128u(u32 a) { return *(const LAS u32x4*)(size_t)a; }
DI void lds_st128(u32 a, bf16x8 v) { *(LAS bf16x8*)(size_t)a = v; }
DI void lds_st128u(u32 a, u32x4 v) { *(LAS u32x4*)(size_t)a = v; }
DI bf16x8 trfrag(u32 a0, u32 a1) {
  bf16x4 lo = __builtin_amdgcn_ds_read_tr16_b64_v4bf16((LAS bf16x4*)(size_t)a0);
  bf16x4 hi = __builtin_amdgcn_ds_read_tr16_b64_v4bf16((LAS bf16x4*)(size_t)a1);
  return __builtin_shufflevector(lo, hi, 0, 1, 2, 3, 4, 5, 6, 7);
}
#define MFMA(a, b, c) __builtin_amdgcn_mfma_f32_32x32x16_bf16((a), (b), (c), 0, 0, 0)
DI float swap_max(float v) { auto r = __builtin_amdgcn_permlane32_swap(__float_as_uint(v), __float_as_uint(v), false, false); return fmaxf(__uint_as_float(r[0]), __uint_as_float(r[1])); }
DI float swap_add(float v) { auto r = __builtin_amdgcn_permlane32_swap(__float_as_uint(v), __float_as_uint(v), false, false); return __uint_as_float(r[0]) + __uint_as_float(r[1]); }

struct Chk { u32 a, b; };
DI void chk_add(Chk& c, u32 d) { c.a = __builtin_amdgcn_alignbit(c.a, c.a, 27) ^ d; c.b = __builtin_amdgcn_alignbit(c.b, c.b, 19) + d; }
DI void chk_add4(Chk& c, u32x4 v) { chk_add(c, v[0]); chk_add(c, v[1]); chk_add(c, v[2]); chk_add(c, v[3]); }
DI Chk chk_init(u64 tag) { return Chk{(u32)tag, (u32)(tag >> 32)}; }
DI u32x4 granule(u64 tag, u32 d0, u32 d1) { Chk c = chk_init(tag); chk_add(c, d0); chk_add(c, d1); chk_add(c, d0 ^ d1); return u32x4{d0, d1, c.a, c.b}; }
DI bool granule_ok(u64 tag, u32x4 v) { const u32x4 e = granule(tag, v[0], v[1]); return e[2] == v[2] && e[3] == v[3]; }
DI void poll_flags(const GAS u64* base, int mask, int lane, u64 tag) {
  const GAS u64* p = base + (lane & mask);
#pragma unroll 1
  for (u32 spins = 0; spins < SPIN_CAP; ++spins) {
    u64 v = __hip_atomic_load(p, RLX_AGENT);
    if (__all(v == tag)) break;
    __builtin_amdgcn_s_sleep(1);
  }
  asm volatile("" ::: "memory");
}

struct St {
  bf16x8 qf[8], kf[8], vf[8];
  f32x16 o[4], sA, sB;
  u32x4 pA[2], pB[2];
  float e[2][2];
  float bs[2];
  float l, mc, mx, al;
  bool pend;
  u32 ak[2], av[2];
};
template <int G> DI void sm_slice(St& c, const f32x16& s, u32x4 (&p)[2]) {
  c.e[(G >> 1) & 1][G & 1] = __builtin_amdgcn_exp2f(fmaf(s[G], CSC, -c.mc));
  if constexpr (G >= 1) { constexpr int H = G - 1; c.bs[H & 1] += c.e[(H >> 1) & 1][H & 1]; PIN(c.bs[H & 1]); }
  if constexpr (G >= 2 && (G & 1) == 0) { constexpr int Q = (G - 2) >> 1; u32 w = cvtpk(c.e[Q & 1][0], c.e[Q & 1][1]); PIN(w); p[Q >> 2][Q & 3] = w; }
}
template <int I> DI bf16x8 ldk1(u32 a0, u32 a1, int kb) { return lds128(((I & 1) ? a1 : a0) + 8192u * kb + 512u * (I >> 1)); }
template <int I> DI bf16x8 ldv1(u32 a0, u32 a1, int kb) { return trfrag(a0 + 8192u * kb + 4096u * (I >> 2) + 512u * (I & 3), a1 + 8192u * kb + 4096u * (I >> 2) + 512u * (I & 3)); }
DI float max3(float a, float b, float c) { float r; asm("v_max3_f32 %0, %1, %2, %3" : "=v"(r) : "v"(a), "v"(b), "v"(c)); return r; }
template <int I, bool QK, int LD, bool SM> DI void qk_gap(St& c, f32x16& snew, const f32x16& sold, u32x4 (&p)[2], u32 a0, u32 a1, int kb) {
  if constexpr (QK) { if constexpr (I == 0) snew = MFMA(c.kf[0], c.qf[0], f32x16{}); else snew = MFMA(c.kf[I], c.qf[I], snew); PIN(snew); }
  if constexpr (LD == 1) c.kf[I] = ldk1<I>(a0, a1, kb);
  if constexpr (LD == 2) c.vf[I] = ldv1<I>(a0, a1, kb);
  if constexpr (SM) {
    if constexpr (I < 2) { sm_slice<6 + 2 * I>(c, sold, p); sm_slice<7 + 2 * I>(c, sold, p); }
    else sm_slice<8 + I>(c, sold, p);
    if constexpr (I == 7) {
      c.bs[1] += c.e[1][1];
      p[1][3] = cvtpk(c.e[1][0], c.e[1][1]);
      c.l += c.bs[0] + c.bs[1];
      c.bs[0] = 0.f; c.bs[1] = 0.f;
    }
  }
  SBAR();
}
template <int I, bool PV, bool LD, bool SM> DI void pv_gap(St& c, const u32x4 (&pc)[2], const f32x16& s, u32x4 (&pn)[2], u32 a0, u32 a1, int kb) {
  if constexpr (PV) { c.o[I & 3] = MFMA(c.vf[I], __builtin_bit_cast(bf16x8, pc[I >> 2]), c.o[I & 3]); PIN(c.o[I & 3]); }
  if constexpr (LD) c.kf[I] = ldk1<I>(a0, a1, kb);
  if constexpr (SM) {
    if constexpr (I == 1) {
      const float m0 = fmaxf(s[0], s[1]);
      float ma = max3(m0, s[2], s[3]), mb = max3(m0, s[4], s[5]);
      ma = max3(ma, s[6], s[7]); mb = max3(mb, s[8], s[9]);
      c.mx = ma; c.al = mb;
    } else if constexpr (I == 2) {
      float ma = max3(c.mx, s[10], s[11]), mb = max3(c.al, s[12], s[13]);
      ma = max3(ma, s[14], s[15]);
      const float mx = fmaxf(ma, mb);
      c.pend = false;
      if (__builtin_expect(__any(fmaf(mx, CSC, -c.mc) > THR), 0)) {
        const float mn = fmaxf(c.mc, swap_max(mx) * CSC);
        c.al = __builtin_amdgcn_exp2f(c.mc - mn);
        c.l *= c.al; c.mc = mn; c.pend = true;
      }
      sm_slice<0>(c, s, pn);
    } else if constexpr (I >= 3) sm_slice<I - 2>(c, s, pn);
    if constexpr (I == 7) {
      if (__builtin_expect(c.pend, 0)) {
#pragma unroll
        for (int d = 0; d < 4; ++d)
#pragma unroll
          for (int i = 0; i < 16; ++i) c.o[d][i] *= c.al;
      }
    }
  }
  SBAR();
}
#define G8(F, ...) F<0 __VA_ARGS__; F<1 __VA_ARGS__; F<2 __VA_ARGS__; F<3 __VA_ARGS__; F<4 __VA_ARGS__; F<5 __VA_ARGS__; F<6 __VA_ARGS__; F<7 __VA_ARGS__;
DI u32 kbuf(u32 L0, int t) { return L0 + KR + (u32)((t + 2) & 3) * TB; }
DI u32 vbuf(u32 L0, int t) { return L0 + VR + (u32)(t & 3) * TB; }
DI u32 tile_id(int u, int sp) { return u < XA ? (u32)(2 * NY + u) : u < XA + NY ? (u32)(sp * NY + u - XA) : (u32)(NY + u); }

extern "C" __global__ __launch_bounds__(512, 2) void gqa_block_fused(Params P) {
  extern __shared__ __attribute__((aligned(16))) char lds_[];
  const int tid = threadIdx.x, lane = tid & 63;
  const int wave = __builtin_amdgcn_readfirstlane(tid >> 6);
  const int bid = blockIdx.x;
  const int cidx = (bid & 7) * 4 + (bid >> 6), b = (bid >> 3) & 7;
  const int pi = cidx >> 4, kvh = (cidx >> 1) & 7, sp = cidx & 1;
  const int g = pi * 8 + b, si = kvh * 2 + sp, wg = g * 16 + si;
  const u32 L0 = (u32)(size_t)lds_;
  char* ws = (char*)P.ws;
  const int h = lane >> 5, r = lane & 31, blk = (lane >> 4) & 1, q4 = (lane >> 2) & 3, p4 = lane & 3;
  const __amdgpu_buffer_rsrc_t sW = __builtin_amdgcn_make_buffer_rsrc(ws, 0, (int)WS_END, 0x00020000);

  if (wave >= 4) {
    const int w = wave - 4;
    const __amdgpu_buffer_rsrc_t sX = __builtin_amdgcn_make_buffer_rsrc((char*)P.x[pi] + (size_t)b * 32768, 0, 32768, 0x00020000);
    const __amdgpu_buffer_rsrc_t sQ = __builtin_amdgcn_make_buffer_rsrc((char*)P.wq[pi] + (size_t)(kvh * 512 + sp * 256) * 2, 0, 511 * 8192 + 512, 0x00020000);
    const __amdgpu_buffer_rsrc_t sO = __builtin_amdgcn_make_buffer_rsrc((char*)P.wo[pi] + (size_t)(kvh * 4 + sp * 2) * 128 * 1024, 0, 262144, 0x00020000);
    const size_t kvo = (size_t)(b * 8 + kvh) * 2048 * 256;
    const __amdgpu_buffer_rsrc_t sKV = __builtin_amdgcn_make_buffer_rsrc((char*)(w < 2 ? P.k[pi] : P.v[pi]) + kvo, 0, 524288, 0x00020000);
    u32 vq[2], vo[2], vk[2];
#pragma unroll
    for (int e = 0; e < 2; ++e) {
      const u32 rm = (2 * e + h) & 3, ch = (u32)r ^ (rm << 2);
      vq[e] = (u32)h * 8192u + 16u * ch;
      vo[e] = (u32)h * 1024u + 256u * (ch >> 3) + 16u * (ch & 7u) + 128u;
    }
#pragma unroll
    for (int i = 0; i < 2; ++i) vk[i] = 256u * (u32)((lane >> 2) & 7) + 64u * (u32)h + 16u * ((u32)(lane & 3) ^ (u32)(2 * i + ((lane >> 4) & 1)));
    const u32 vw = (u32)(lane >> 3) * 1024u + 16u * ((u32)(lane & 7) ^ (u32)(((lane >> 4) & 1) << 2));

    auto issue_x = [&]() {
#pragma unroll
      for (int i = 0; i < 8; ++i) { const u32 q = 8u * w + i; dma(L0 + RING + 1024u * q, (16u * lane) ^ (16u * (q & 15u)), sX, 1024u * q); }
    };
    auto issue_wq = [&](int step) {
      const u32 c = (u32)(step - 1 + b) & 7u, buf = L0 + RING + (u32)(step & 3) * BUFB;
#pragma unroll
      for (int i = 0; i < 8; ++i) { const u32 I = 8u * w + i; dma(buf + 1024u * I, vq[i & 1], sQ, (64u * c + 2u * I) * 8192u); }
    };
    auto issue_wol = [&](int cc, u32 dst) {
      const u32 c = (u32)(cc + b) & 3u, buf = L0 + dst;
#pragma unroll
      for (int i = 0; i < 8; ++i) { const u32 I = 8u * w + i; dma(buf + 1024u * I, vo[i & 1], sO, (64u * c + 2u * I) * 1024u); }
    };
    auto issue_w = [&](int cc) {
      const u32 c = (u32)(cc + b) & 3u;
#pragma unroll
      for (int i = 0; i < 8; ++i) dma(L0 + SCR + 8192u * w + 1024u * i, vw, sO, (64u * c + 8u * i) * 1024u + 256u * w);
    };
    const int isv = w >> 1;
    auto issue_kv = [&](int u) {
      const u32 buf = (isv ? vbuf(L0, u) : kbuf(L0, u)) + 8192u * (w & 1), so = tile_id(u, sp) * 16384u + 8192u * (w & 1);
#pragma unroll
      for (int i = 0; i < 8; ++i) dma_n(buf + 1024u * i, vk[(i >> 1) & 1], sKV, so + 2048u * (i >> 1) + 128u * (i & 1));
    };
    auto issue = [&](int step) { if (step == 0) issue_x(); else issue_wq(step); };
    bf16x8 wr[2][16];
    const u32 aw0 = L0 + SCR + 8192u * w + 128u * (4u * h + q4) + 16u * ((2u * blk + (p4 >> 1)) ^ (u32)((q4 >> 1) << 2)) + 8u * (p4 & 1);
    auto read_w = [&](int cc) {
#pragma unroll
      for (int kk = 0; kk < 4; ++kk)
#pragma unroll
        for (int T = 0; T < 2; ++T) { const u32 a = (aw0 ^ (64u * T)) + 2048u * kk; wr[T][4 * cc + kk] = trfrag(a, a + 1024u); }
      asm volatile("s_waitcnt lgkmcnt(0)" ::: "memory");
    };
    issue(0); issue(1); issue(2); issue(3);
    waitvm<24>(); BAR();
#pragma unroll
    for (int i = 1; i <= 5; ++i) { waitvm<16>(); BAR(); issue(i + 3); }
    waitvm<16>(); BAR(); if (!isv) { issue_kv(0); issue_kv(1); }
    if (isv) waitvm<8>(); else waitvm<24>();
    BAR(); if (isv) { issue_kv(0); issue_kv(1); }
    if (isv) waitvm<16>(); else waitvm<16>();
    BAR(); if (isv) { issue_kv(2); issue_kv(3); }
    BAR(); if (!isv) { issue_kv(2); issue_kv(3); }
    waitvm<24>(); BAR(); issue_w(0);
#pragma unroll
    for (int i = 1; i <= 9; ++i) {
      if (i == 3 || i == 5 || i == 7) waitvm<32>(); else waitvm<24>();
      BAR();
      if (i + 3 - isv >= 4) issue_kv(i + 3 - isv);
      if ((i & 1) == 0) { if (i == 2 && isv) waitvm<8>(); else waitvm<16>(); read_w((i >> 1) - 1); if (i < 8) issue_w(i >> 1); }
    }
    for (int i = 10; i <= NT - 3; ++i) { waitvm<16>(); BAR(); if (i + 3 - isv < NT) issue_kv(i + 3 - isv); }
    waitvm<8>(); BAR(); issue_wol(0, RING + 1 * BUFB);
    waitvm<8>(); BAR(); issue_wol(1, RING + 2 * BUFB);
    waitvm<16>(); BAR(); issue_wol(2, RING + 0 * BUFB);
    BAR(); issue_wol(3, RING + 3 * BUFB);
    DRAIN(); BAR();
    const u64 tag = *(volatile LAS u64*)(size_t)(L0 + MISC);
    f32x16 acc[2] = {f32x16{}, f32x16{}};
    {
      bf16x8 B[16];
#pragma unroll
      for (int j = 0; j < 16; ++j) B[j] = lds128(L0 + SCR + 1024u * (4u * ((u32)((j >> 2) + b) & 3u) + (j & 3)) + 16u * lane);
#pragma unroll
      for (int j = 0; j < 16; ++j) { acc[0] = MFMA(wr[0][j], B[j], acc[0]); acc[1] = MFMA(wr[1][j], B[j], acc[1]); }
    }
#pragma unroll
    for (int n = 0; n < 2; ++n)
#pragma unroll
      for (int u = 0; u < 2; ++u)
        __builtin_amdgcn_raw_buffer_store_b128(__builtin_bit_cast(u32x4, pack8(acc[n], u)), sW, 1024u * u + 16u * lane + WS_SL + (u32)((g * 16 + 4 * w + n) * 16 + si) * 2048u, 0, AUX_SC1);
    DRAIN();
    if (lane == 0) __hip_atomic_store((GAS u64*)(ws + WS_F3 + (g * 8 + wave) * 128) + si, tag, RLX_AGENT);
    return;
  }

  const int w = wave;
  const u64 tag = __hip_atomic_load((GAS u64*)(ws + WS_EP + g * 256), RLX_AGENT) + TAGC;
  BAR();
  St c;
  {
    bf16x8 xf[32];
#pragma unroll
    for (int j = 0; j < 32; ++j) {
      const u32 ks = 4u * ((u32)((j >> 2) + b) & 7u) + (j & 3);
      xf[j] = lds128(L0 + RING + 1024u * r + ((32u * ks + 16u * h) ^ (16u * (r & 15))));
    }
    f32x16 qa[2] = {f32x16{}, f32x16{}};
    const u32 aq0 = 512u * (8u * h + q4) + 16u * ((8u * w + 2u * blk + (p4 & 1)) ^ (4u * q4)) + 8u * (p4 >> 1);
#pragma unroll
    for (int cc = 0; cc < 8; ++cc) {
      BAR();
      const u32 buf = L0 + RING + (u32)((cc + 1) & 3) * BUFB;
      bf16x8 af[8];
#pragma unroll
      for (int kk = 0; kk < 4; ++kk)
#pragma unroll
        for (int T = 0; T < 2; ++T) { const u32 a = buf + (aq0 ^ (64u * T)) + 8192u * kk; af[2 * kk + T] = trfrag(a, a + 2048u); }
      SBAR();
#pragma unroll
      for (int kk = 0; kk < 4; ++kk)
#pragma unroll
        for (int T = 0; T < 2; ++T) qa[T] = MFMA(af[2 * kk + T], xf[4 * cc + kk], qa[T]);
    }
    const u32 fo = (u32)((w >> 1) * 8 + 4 * (w & 1)) * 1024u + 16u * lane;
    Chk ck = chk_init(tag);
#pragma unroll
    for (int T = 0; T < 2; ++T)
#pragma unroll
      for (int s = 0; s < 2; ++s) {
        const bf16x8 f = pack8(qa[T], s);
        lds_st128(L0 + SCR + fo + 1024u * (2 * T + s), f);
        __builtin_amdgcn_raw_buffer_store_b128(__builtin_bit_cast(u32x4, f), sW, 1024u * (2 * T + s) + 16u * lane + WS_QX + (u32)(wg * 4 + w) * QXW, 0, AUX_SC1);
        chk_add4(ck, __builtin_bit_cast(u32x4, f));
      }
    __builtin_amdgcn_raw_buffer_store_b64(u32x2{ck.a, ck.b}, sW, 4096u + 8u * lane + WS_QX + (u32)(wg * 4 + w) * QXW, 0, AUX_SC1);
  }
  const bool own = (w >> 1) == sp;
  const int hl = w & 1;
  BAR();
  if (own) {
#pragma unroll
    for (int ks = 0; ks < 8; ++ks) c.qf[ks] = lds128(L0 + SCR + (u32)(hl * 8 + ks) * 1024u + 16u * lane);
  } else {
#pragma unroll
    for (int k = 0; k <= XA - 2; ++k) BAR();
    u32x4 f[8]; u32x2 cv[2];
    auto fetch = [&]() {
#pragma unroll
      for (int pw = 0; pw < 2; ++pw) {
        const u32 so = WS_QX + (u32)((wg ^ 1) * 4 + 2 * hl + pw) * QXW;
#pragma unroll
        for (int j = 0; j < 4; ++j) f[4 * pw + j] = __builtin_amdgcn_raw_buffer_load_b128(sW, 1024u * j + 16u * lane, so, AUX_SC1);
        cv[pw] = __builtin_amdgcn_raw_buffer_load_b64(sW, 4096u + 8u * lane, so, AUX_SC1);
      }
    };
    fetch();
    BAR();
#pragma unroll 1
    for (u32 spins = 0; spins < SPIN_CAP; ++spins) {
      bool ok = true;
#pragma unroll
      for (int pw = 0; pw < 2; ++pw) {
        Chk c2 = chk_init(tag);
#pragma unroll
        for (int j = 0; j < 4; ++j) chk_add4(c2, f[4 * pw + j]);
        ok = ok && c2.a == cv[pw][0] && c2.b == cv[pw][1];
      }
      if (__all(ok)) break;
      __builtin_amdgcn_s_sleep(1);
      fetch();
    }
#pragma unroll
    for (int ks = 0; ks < 8; ++ks) c.qf[ks] = __builtin_bit_cast(bf16x8, f[ks]);
  }
#pragma unroll
  for (int e = 0; e < 2; ++e) {
    c.ak[e] = 2048u * (u32)(r >> 3) + 64u * (u32)(r & 7) + 16u * ((u32)(2 * e + h) ^ (u32)((r >> 2) & 3));
    c.av[e] = 2048u * e + 64u * (u32)(4 * h + q4) + 16u * ((u32)(2 * blk + (p4 >> 1)) ^ (u32)((2 * e + h) & 3)) + 8u * (u32)(p4 & 1);
  }
#pragma unroll
  for (int d = 0; d < 4; ++d) c.o[d] = f32x16{};
  c.l = 0.f; c.bs[0] = c.bs[1] = 0.f;
  const int t0 = own ? 0 : XA, t1 = own ? NT : XA + NY;
  c.mc = -__builtin_inff(); c.pend = false; c.al = 1.f; c.mx = 0.f;
  BAR();
  {
    u32 a0 = kbuf(L0, t0) + c.ak[0], a1 = kbuf(L0, t0) + c.ak[1];
    asm("" : "+v"(a0)); asm("" : "+v"(a1));
#pragma unroll
    for (int i = 0; i < 8; ++i) c.kf[i] = lds128(((i & 1) ? a1 : a0) + 512u * (i >> 1));
    G8(qk_gap, , true, 1, false>(c, c.sA, c.sA, c.pA, a0, a1, 1))
    asm volatile("s_nop 7\n\ts_nop 7" ::: "memory");
    G8(pv_gap, , false, false, true>(c, c.pA, c.sA, c.pA, 0u, 0u, 0))
  }
#pragma unroll 1
  for (int t = t0; t < t1 - 1; ++t) {
    u32 kn0 = kbuf(L0, t + 1) + c.ak[0], kn1 = kbuf(L0, t + 1) + c.ak[1], vc0 = vbuf(L0, t) + c.av[0], vc1 = vbuf(L0, t) + c.av[1];
    asm("" : "+v"(kn0)); asm("" : "+v"(kn1)); asm("" : "+v"(vc0)); asm("" : "+v"(vc1));
    BAR();
    G8(qk_gap, , true, 2, true>(c, c.sB, c.sA, c.pA, vc0, vc1, 0))
    G8(pv_gap, , true, true, true>(c, c.pA, c.sB, c.pB, kn0, kn1, 0))
    G8(qk_gap, , true, 2, true>(c, c.sA, c.sB, c.pB, vc0, vc1, 1))
    G8(pv_gap, , true, true, true>(c, c.pB, c.sA, c.pA, kn0, kn1, 1))
  }
  {
    u32 vc0 = vbuf(L0, t1 - 1) + c.av[0], vc1 = vbuf(L0, t1 - 1) + c.av[1];
    BAR();
    G8(qk_gap, , true, 2, true>(c, c.sB, c.sA, c.pA, vc0, vc1, 0))
    G8(pv_gap, , true, false, true>(c, c.pA, c.sB, c.pB, 0u, 0u, 0))
    G8(qk_gap, , false, 2, true>(c, c.sA, c.sB, c.pB, vc0, vc1, 1))
    if (own) BAR();
    G8(pv_gap, , true, false, false>(c, c.pB, c.sA, c.pA, 0u, 0u, 0))
  }
  const float l = swap_add(c.l), mc = c.mc;
  if (!own) {
    {
      const u32 ob = WS_OX + (u32)(wg * 2 + hl) * OXH;
      const float inv = 1.0f / l;
      Chk ck = chk_init(tag);
#pragma unroll
      for (int d = 0; d < 4; ++d) {
#pragma unroll
        for (int i = 0; i < 16; ++i) c.o[d][i] *= inv;
#pragma unroll
        for (int s2 = 0; s2 < 2; ++s2) {
          const u32x4 f = __builtin_bit_cast(u32x4, pack8(c.o[d], s2));
          __builtin_amdgcn_raw_buffer_store_b128(f, sW, 1024u * (2 * d + s2) + 16u * lane + ob, 0, AUX_SC1);
          chk_add4(ck, f);
        }
      }
      chk_add(ck, __float_as_uint(mc)); chk_add(ck, __float_as_uint(l));
      __builtin_amdgcn_raw_buffer_store_b128(u32x4{__float_as_uint(mc), __float_as_uint(l), ck.a, ck.b}, sW, 8192u + 16u * lane + ob, 0, AUX_SC1);
    }
#pragma unroll
    for (int k = 0; k < XB - 1; ++k) BAR();
    const u32 ob = WS_OX + (u32)((wg ^ 1) * 2 + hl) * OXH;
    u32x4 f[8], st;
    auto fetch = [&]() {
#pragma unroll
      for (int j = 0; j < 8; ++j) f[j] = __builtin_amdgcn_raw_buffer_load_b128(sW, 1024u * j + 16u * lane, ob, AUX_SC1);
      st = __builtin_amdgcn_raw_buffer_load_b128(sW, 8192u + 16u * lane, ob, AUX_SC1);
    };
    fetch();
    BAR();
#pragma unroll 1
    for (u32 spins = 0; spins < SPIN_CAP; ++spins) {
      Chk c2 = chk_init(tag);
#pragma unroll
      for (int j = 0; j < 8; ++j) chk_add4(c2, f[j]);
      chk_add(c2, st[0]); chk_add(c2, st[1]);
      if (__all(c2.a == st[2] && c2.b == st[3])) break;
      __builtin_amdgcn_s_sleep(1);
      fetch();
    }
#pragma unroll
    for (int j = 0; j < 8; ++j) lds_st128u(L0 + SCR + (u32)(hl * 8 + j) * 1024u + 16u * lane, f[j]);
    if (lane < 32) *(LAS u32x2*)(size_t)(L0 + PST + 256u * hl + 8u * lane) = u32x2{st[0], st[1]};
    if (hl == 0 && lane == 0) *(volatile LAS u64*)(size_t)(L0 + MISC) = tag;
    BAR();
  } else {
    const u32x2 st = *(const LAS u32x2*)(size_t)(L0 + PST + 256u * hl + 8u * r);
    u32x4 f[8];
#pragma unroll
    for (int j = 0; j < 8; ++j) f[j] = lds128u(L0 + SCR + (u32)(hl * 8 + j) * 1024u + 16u * lane);
    const float m2 = __uint_as_float(st[0]), lp = __uint_as_float(st[1]);
    const float mn = fmaxf(mc, m2);
    const float a1 = __builtin_amdgcn_exp2f(mc - mn), a2 = __builtin_amdgcn_exp2f(m2 - mn) * lp;
    const float inv = 1.0f / (l * a1 + a2);
    const float f1 = a1 * inv, f2 = a2 * inv;
#pragma unroll
    for (int j = 0; j < 8; ++j) {
      u32x4 o4;
#pragma unroll
      for (int e = 0; e < 4; ++e) {
        const float lo = c.o[j >> 1][8 * (j & 1) + 2 * e] * f1 + __uint_as_float(f[j][e] << 16) * f2;
        const float hi = c.o[j >> 1][8 * (j & 1) + 2 * e + 1] * f1 + __uint_as_float(f[j][e] & 0xffff0000u) * f2;
        o4[e] = cvtpk(lo, hi);
      }
      lds_st128u(L0 + SCR + (u32)(hl * 8 + j) * 1024u + 16u * lane, o4);
    }
  }
  BAR();
  {
    f32x16 acc[2] = {f32x16{}, f32x16{}};
    const u32 ao0 = 512u * (4u * h + q4) + 16u * ((8u * w + 2u * blk + (p4 >> 1)) ^ (4u * q4)) + 8u * (p4 & 1);
#pragma unroll
    for (int cc = 0; cc < 4; ++cc) {
      bf16x8 B[4], A[8];
      const u32 buf = L0 + RING + (cc == 0 ? 1u : cc == 1 ? 2u : cc == 2 ? 0u : 3u) * BUFB;
#pragma unroll
      for (int kk = 0; kk < 4; ++kk) {
        B[kk] = lds128(L0 + SCR + 1024u * (4u * ((u32)(cc + b) & 3u) + kk) + 16u * lane);
#pragma unroll
        for (int T = 0; T < 2; ++T) { const u32 a = buf + (ao0 ^ (64u * T)) + 8192u * kk; A[2 * kk + T] = trfrag(a, a + 4096u); }
      }
#pragma unroll
      for (int kk = 0; kk < 4; ++kk) { acc[0] = MFMA(A[2 * kk], B[kk], acc[0]); acc[1] = MFMA(A[2 * kk + 1], B[kk], acc[1]); }
    }
#pragma unroll
    for (int n = 0; n < 2; ++n)
#pragma unroll
      for (int u = 0; u < 2; ++u)
        __builtin_amdgcn_raw_buffer_store_b128(__builtin_bit_cast(u32x4, pack8(acc[n], u)), sW, 1024u * u + 16u * lane + WS_SL + (u32)((g * 16 + 4 * w + 2 + n) * 16 + si) * 2048u, 0, AUX_SC1);
    DRAIN();
    if (lane == 0) __hip_atomic_store((GAS u64*)(ws + WS_F3 + (g * 8 + wave) * 128) + si, tag, RLX_AGENT);
  }
  poll_flags((const GAS u64*)(ws + WS_F3 + (g * 8 + (si >> 2) + ((si & 2) ? 0 : 4)) * 128), 15, lane, tag);
  f32x4 sum = {0.f, 0.f, 0.f, 0.f};
  {
    u32x2 v[16];
#pragma unroll
    for (int i = 0; i < 16; ++i)
      v[i] = __builtin_amdgcn_raw_buffer_load_b64(sW, 1024u * (w >> 1) + 16u * lane + 8u * (w & 1) + 2048u * i, WS_SL + (u32)((g * 16 + si) * 16) * 2048u, AUX_SC1);
#pragma unroll
    for (int i = 0; i < 16; ++i) {
      sum[0] += __uint_as_float(v[i][0] << 16); sum[1] += __uint_as_float(v[i][0] & 0xffff0000u);
      sum[2] += __uint_as_float(v[i][1] << 16); sum[3] += __uint_as_float(v[i][1] & 0xffff0000u);
    }
  }
  u32x2 ov = {cvtpk(sum[0], sum[1]), cvtpk(sum[2], sum[3])};
  *(GAS u32x2*)((char*)P.out + ((size_t)(b * 32 + r) * 1024 + pi * 512 + 32 * si + 8 * w + 4 * h) * 2) = ov;
  if (w == 0 && lane == 0) __hip_atomic_store((GAS u64*)(ws + WS_EP + g * 256), tag, RLX_AGENT);
}

extern "C" void kernel_launch(void* const* d_in, const int* in_sizes, int n_in, void* d_out, int out_size, void* d_ws, size_t ws_size, hipStream_t stream) {
  (void)hipFuncSetAttribute((const void*)gqa_block_fused, hipFuncAttributeMaxDynamicSharedMemorySize, (int)LDS_BYTES);
  Params P;
  for (int p = 0; p < 2; ++p) { P.x[p] = d_in[5 * p + 0]; P.k[p] = d_in[5 * p + 1]; P.v[p] = d_in[5 * p + 2]; P.wq[p] = d_in[5 * p + 3]; P.wo[p] = d_in[5 * p + 4]; }
  P.out = d_out; P.ws = d_ws;
  hipLaunchKernelGGL(gqa_block_fused, dim3(256), dim3(512), LDS_BYTES, stream, P);
}
```

```cpp
#include <hip/hip_runtime.h>
#include <stdint.h>
#include <type_traits>

typedef unsigned u32;
typedef unsigned long long u64;
typedef u32 u32x4 __attribute__((ext_vector_type(4)));
typedef u32 u32x2 __attribute__((ext_vector_type(2)));
typedef float f32x2 __attribute__((ext_vector_type(2)));
typedef float f32x4 __attribute__((ext_vector_type(4)));
typedef float f32x16 __attribute__((ext_vector_type(16)));
typedef __bf16 bf16x2 __attribute__((ext_vector_type(2)));
typedef __bf16 bf16x4 __attribute__((ext_vector_type(4)));
typedef __bf16 bf16x8 __attribute__((ext_vector_type(8)));
#define LAS __attribute__((address_space(3)))
#define GAS __attribute__((address_space(1)))
#define DI __device__ __forceinline__
#define RLX_AGENT __ATOMIC_RELAXED, __HIP_MEMORY_SCOPE_AGENT
#define SBAR() __builtin_amdgcn_sched_barrier(0)
#define PIN(x) asm volatile("" : "+v"(x))

constexpr u32 KR = 0, VR = 65536, TB = 16384, EX = 131072, LDS_BYTES = 163840;
constexpr u32 MS = 98304;
constexpr u32 MISC = MS + 6144;
constexpr int NT = 32, NREG = 6;
constexpr u32 WS_EP = 0;
constexpr u32 WS_F3 = 4096;
constexpr u32 WS_SL = 32768;
constexpr u32 WS_END = WS_SL + 16u * 16u * 16u * 2048u;
constexpr u64 TAGC = 0x9E3779B97F4A7C15ull;
constexpr float CSC = 0.088388347648318447f * 1.4426950408889634f;
constexpr float THR = 8.0f;
constexpr int AUX_SC1 = 16, AUX_NT = 2;
constexpr u32 SPIN_CAP = 1u << 18;

struct Params {
  const void* x[2]; const void* k[2]; const void* v[2]; const void* wq[2]; const void* wo[2];
  void* out; void* ws;
};

#define BAR() asm volatile("s_waitcnt lgkmcnt(0)\n\ts_barrier" ::: "memory")
#define DRAIN() asm volatile("s_waitcnt vmcnt(0)" ::: "memory")
#define LGKM0() asm volatile("s_waitcnt lgkmcnt(0)" ::: "memory")
template <int N> DI void waitvm() { asm volatile("s_waitcnt vmcnt(%0)" ::"i"(N) : "memory"); }

DI void dma(u32 m0v, u32 voff, __amdgpu_buffer_rsrc_t srd, u32 soff) {
  asm volatile("s_mov_b32 m0, %0\n\ts_nop 0\n\tbuffer_load_dwordx4 %1, %2, %3 offen nt lds" ::"s"(m0v), "v"(voff), "s"(srd), "s"(soff) : "memory");
}
DI void dma_t(u32 m0v, u32 voff, __amdgpu_buffer_rsrc_t srd, u32 soff) {
  asm volatile("s_mov_b32 m0, %0\n\ts_nop 0\n\tbuffer_load_dwordx4 %1, %2, %3 offen lds" ::"s"(m0v), "v"(voff), "s"(srd), "s"(soff) : "memory");
}
#define DMAW dma_t
DI u32 cvtpk(float lo, float hi) { f32x2 v = {lo, hi}; bf16x2 b = __builtin_convertvector(v, bf16x2); return __builtin_bit_cast(u32, b); }
DI bf16x8 pack8(const f32x16& x, int s) {
  u32x4 w = {cvtpk(x[8 * s], x[8 * s + 1]), cvtpk(x[8 * s + 2], x[8 * s + 3]), cvtpk(x[8 * s + 4], x[8 * s + 5]), cvtpk(x[8 * s + 6], x[8 * s + 7])};
  return __builtin_bit_cast(bf16x8, w);
}
DI bf16x8 lds128(u32 a) { return *(const LAS bf16x8*)(size_t)a; }
DI u32x4 lds128u(u32 a) { return *(const LAS u32x4*)(size_t)a; }
DI void lds_st128(u32 a, bf16x8 v) { *(LAS bf16x8*)(size_t)a = v; }
DI void lds_st128u(u32 a, u32x4 v) { *(LAS u32x4*)(size_t)a = v; }
DI bf16x8 trfrag(u32 a0, u32 a1) {
  bf16x4 lo = __builtin_amdgcn_ds_read_tr16_b64_v4bf16((LAS bf16x4*)(size_t)a0);
  bf16x4 hi = __builtin_amdgcn_ds_read_tr16_b64_v4bf16((LAS bf16x4*)(size_t)a1);
  return __builtin_shufflevector(lo, hi, 0, 1, 2, 3, 4, 5, 6, 7);
}
#define MFMA(a, b, c) __builtin_amdgcn_mfma_f32_32x32x16_bf16((a), (b), (c), 0, 0, 0)
DI float swap_max(float v) { auto r = __builtin_amdgcn_permlane32_swap(__float_as_uint(v), __float_as_uint(v), false, false); return fmaxf(__uint_as_float(r[0]), __uint_as_float(r[1])); }
DI float swap_add(float v) { auto r = __builtin_amdgcn_permlane32_swap(__float_as_uint(v), __float_as_uint(v), false, false); return __uint_as_float(r[0]) + __uint_as_float(r[1]); }
DI float max3(float a, float b, float c) { float r; asm("v_max3_f32 %0, %1, %2, %3" : "=v"(r) : "v"(a), "v"(b), "v"(c)); return r; }

DI void poll_flags(const GAS u64* base, int mask, int lane, u64 tag) {
  const GAS u64* p = base + (lane & mask);
#pragma unroll 1
  for (u32 spins = 0; spins < SPIN_CAP; ++spins) {
    u64 v = __hip_atomic_load(p, RLX_AGENT);
    if (__all(v == tag)) break;
    __builtin_amdgcn_s_sleep(1);
  }
  asm volatile("" ::: "memory");
}

struct St {
  bf16x8 qf[8], kf[8], vf[8];
  f32x16 o[4], sA, sB;
  u32x4 p[2];
  float e[2][2];
  float bs[2];
  float l, mc, mx, al;
  bool pend;
  u32 ak[2], av[2];
};
template <int G> DI void sm_slice(St& c, const f32x16& s) {
  c.e[(G >> 1) & 1][G & 1] = __builtin_amdgcn_exp2f(fmaf(s[G], CSC, -c.mc));
  if constexpr (G >= 1) { constexpr int H = G - 1; c.bs[H & 1] += c.e[(H >> 1) & 1][H & 1]; PIN(c.bs[H & 1]); }
  if constexpr (G >= 2 && (G & 1) == 0) { constexpr int Q = (G - 2) >> 1; u32 w = cvtpk(c.e[Q & 1][0], c.e[Q & 1][1]); PIN(w); c.p[Q >> 2][Q & 3] = w; }
}
DI void sm_tail(St& c) {
  c.bs[1] += c.e[1][1];
  c.p[1][3] = cvtpk(c.e[1][0], c.e[1][1]);
  c.l += c.bs[0] + c.bs[1];
  c.bs[0] = 0.f; c.bs[1] = 0.f;
}
template <int I> DI bf16x8 ldk1(u32 a0, u32 a1) { return lds128(((I & 1) ? a1 : a0) + 512u * (I >> 1)); }
template <int I> DI bf16x8 ldv1(u32 a0, u32 a1) { return trfrag(a0 + 4096u * (I >> 2) + 512u * (I & 3), a1 + 4096u * (I >> 2) + 512u * (I & 3)); }
template <int I, bool QK> DI void qk_gap(St& c, f32x16& snew, const f32x16& scur, u32 v0, u32 v1) {
  if constexpr (QK) { if constexpr (I == 0) snew = MFMA(c.kf[0], c.qf[0], f32x16{}); else snew = MFMA(c.kf[I], c.qf[I], snew); PIN(snew); }
  c.vf[I] = ldv1<I>(v0, v1);
  if constexpr (I < 4) { sm_slice<4 + 2 * I>(c, scur); sm_slice<5 + 2 * I>(c, scur); }
  else sm_slice<8 + I>(c, scur);
  if constexpr (I == 7) sm_tail(c);
  SBAR();
}
template <int I, bool MX> DI void pv_gap(St& c, const f32x16& s) {
  c.o[I & 3] = MFMA(c.vf[I], __builtin_bit_cast(bf16x8, c.p[I >> 2]), c.o[I & 3]); PIN(c.o[I & 3]);
  if constexpr (MX) {
    if constexpr (I == 2) {
      const float m0 = fmaxf(s[0], s[1]);
      float ma = max3(m0, s[2], s[3]), mb = max3(m0, s[4], s[5]);
      ma = max3(ma, s[6], s[7]); mb = max3(mb, s[8], s[9]);
      c.mx = ma; c.al = mb;
    } else if constexpr (I == 3) {
      float ma = max3(c.mx, s[10], s[11]), mb = max3(c.al, s[12], s[13]);
      ma = max3(ma, s[14], s[15]);
      c.mx = fmaxf(ma, mb);
    } else if constexpr (I == 4) {
      c.pend = false;
      if (__builtin_expect(__any(fmaf(c.mx, CSC, -c.mc) > THR), 0)) {
        const float mn = fmaxf(c.mc, swap_max(c.mx) * CSC);
        c.al = __builtin_amdgcn_exp2f(c.mc - mn);
        c.l *= c.al; c.mc = mn; c.pend = true;
      }
    } else if constexpr (I == 7) {
      if (__builtin_expect(c.pend, 0)) {
#pragma unroll
        for (int d = 0; d < 4; ++d)
#pragma unroll
          for (int i = 0; i < 16; ++i) c.o[d][i] *= c.al;
      }
    }
  }
  SBAR();
}
#define G8(F, ...) F<0 __VA_ARGS__; F<1 __VA_ARGS__; F<2 __VA_ARGS__; F<3 __VA_ARGS__; F<4 __VA_ARGS__; F<5 __VA_ARGS__; F<6 __VA_ARGS__; F<7 __VA_ARGS__;
template <bool NEXT> DI void tile_step(St& c, f32x16& scur, f32x16& snew, u32 L0, int t) {
  u32 k0 = L0 + KR + (u32)((t + 1) & 3) * TB + c.ak[0], k1 = L0 + KR + (u32)((t + 1) & 3) * TB + c.ak[1];
  u32 v0 = L0 + VR + (u32)(t & 3) * TB + c.av[0], v1 = L0 + VR + (u32)(t & 3) * TB + c.av[1];
  asm("" : "+v"(k0)); asm("" : "+v"(k1)); asm("" : "+v"(v0)); asm("" : "+v"(v1));
  BAR();
  if constexpr (NEXT) {
    c.kf[0] = ldk1<0>(k0, k1); c.kf[1] = ldk1<1>(k0, k1); c.kf[2] = ldk1<2>(k0, k1); c.kf[3] = ldk1<3>(k0, k1);
    c.kf[4] = ldk1<4>(k0, k1); c.kf[5] = ldk1<5>(k0, k1); c.kf[6] = ldk1<6>(k0, k1); c.kf[7] = ldk1<7>(k0, k1);
  }
  SBAR();
  sm_slice<0>(c, scur); sm_slice<1>(c, scur); sm_slice<2>(c, scur); sm_slice<3>(c, scur);
  SBAR();
  G8(qk_gap, , NEXT>(c, snew, scur, v0, v1))
  if constexpr (!NEXT) BAR();
  G8(pv_gap, , NEXT>(c, snew))
}

extern "C" __global__ __launch_bounds__(512, 2) void gqa_block_fused(Params P) {
  extern __shared__ __attribute__((aligned(16))) char lds_[];
  const int tid = threadIdx.x, lane = tid & 63;
  const int wave = __builtin_amdgcn_readfirstlane(tid >> 6);
  const int bid = blockIdx.x;
  const int cidx = (bid & 7) * 4 + (bid >> 6), b = (bid >> 3) & 7;
  const int pi = cidx >> 4, kvh = (cidx >> 1) & 7, hh = cidx & 1;
  const int g = pi * 8 + b, si = kvh * 2 + hh;
  const u32 L0 = (u32)(size_t)lds_;
  char* ws = (char*)P.ws;
  const int h = lane >> 5, r = lane & 31, blk = (lane >> 4) & 1, q4 = (lane >> 2) & 3, p4 = lane & 3;
  const __amdgpu_buffer_rsrc_t sW = __builtin_amdgcn_make_buffer_rsrc(ws, 0, (int)WS_END, 0x00020000);
  auto ofrag = [&](u32 ks) -> u32 { const u32 dt = (ks >> 1) & 3u, wo_ = (ks >> 3) + 2u * (dt >> 1); return L0 + MS + 8192u * (wo_ ^ 2u) + 1024u * (2u * (dt & 1u) + (ks & 1u)) + 16u * lane; };

  if (wave >= 4) {
    const int w = wave - 4;
    const size_t kvo = (size_t)(b * 8 + kvh) * 2048 * 256;
    const __amdgpu_buffer_rsrc_t sKV = __builtin_amdgcn_make_buffer_rsrc((char*)(w < 2 ? P.k[pi] : P.v[pi]) + kvo, 0, 524288, 0x00020000);
    const __amdgpu_buffer_rsrc_t sO = __builtin_amdgcn_make_buffer_rsrc((char*)P.wo[pi] + (size_t)(kvh * 4 + hh * 2) * 128 * 1024, 0, 262144, 0x00020000);
    u32 vk[2], vo[2];
#pragma unroll
    for (int i = 0; i < 2; ++i) {
      vk[i] = 256u * (u32)((lane >> 2) & 7) + 64u * (u32)h + 16u * ((u32)(lane & 3) ^ (u32)(2 * i + ((lane >> 4) & 1))) + 8192u * (u32)(w & 1);
      const u32 rm = (2 * i + h) & 3, ch = (u32)r ^ (rm << 2);
      vo[i] = (u32)h * 1024u + 256u * (ch >> 3) + 16u * (ch & 7u) + 128u;
    }
    const u32 vw = (u32)(lane >> 3) * 1024u + 16u * ((u32)(lane & 7) ^ (u32)(((lane >> 4) & 1) << 2)) + 256u * (u32)w;
    const u32 rb = L0 + (w < 2 ? KR : VR) + 8192u * (u32)(w & 1);
    u32x4 st[NREG][8];
    auto load_item = [&](int i) {
#pragma unroll
      for (int j = 0; j < 8; ++j) st[i][j] = __builtin_amdgcn_raw_buffer_load_b128(sKV, vk[(j >> 1) & 1] + 2048u * (j >> 1) + 128u * (j & 1), (u32)i * 16384u, AUX_NT);
      SBAR();
    };
    auto write_tile = [&](int i) {
#pragma unroll
      for (int j = 0; j < 8; ++j) lds_st128u(rb + (u32)(i & 3) * TB + 1024u * j + 16u * lane, st[i][j]);
      SBAR();
    };
    auto dma_tile = [&](int u) {
#pragma unroll
      for (int j = 0; j < 8; ++j) dma(rb + (u32)(u & 3) * TB + 1024u * j, vk[(j >> 1) & 1], sKV, (u32)u * 16384u + 2048u * (j >> 1) + 128u * (j & 1));
    };
    bf16x8 wr[2][16];
    const u32 aw0 = L0 + EX + 8192u * w + 128u * (4u * h + q4) + 16u * ((2u * blk + (p4 >> 1)) ^ (u32)((q4 >> 1) << 2)) + 8u * (p4 & 1);
    auto issue_w = [&](int cc) {
      const u32 c = (u32)(cc + b) & 3u;
#pragma unroll
      for (int i = 0; i < 8; ++i) DMAW(L0 + EX + 8192u * w + 1024u * i, vw, sO, (64u * c + 8u * i) * 1024u);
    };
    auto read_w = [&](int cc) {
#pragma unroll
      for (int kk = 0; kk < 4; ++kk)
#pragma unroll
        for (int T = 0; T < 2; ++T) { const u32 a = (aw0 ^ (64u * T)) + 2048u * kk; wr[T][4 * cc + kk] = trfrag(a, a + 1024u); }
      LGKM0();
      SBAR();
    };
    auto issue_wol = [&](int cc, u32 dst) {
      const u32 c = (u32)(cc + b) & 3u;
#pragma unroll
      for (int i = 0; i < 8; ++i) { const u32 I = 8u * w + i; DMAW(L0 + dst + 1024u * I, vo[i & 1], sO, (64u * c + 2u * I) * 1024u); }
    };
    BAR();
    load_item(0); load_item(1); load_item(2);
    BAR(); BAR(); BAR();
    load_item(3); load_item(4); load_item(5);
    BAR();
    write_tile(0); write_tile(1); write_tile(2); write_tile(3);
    BAR();
    auto stream = [&](auto offc) {
      constexpr int OFF = decltype(offc)::value;
#pragma unroll
      for (int t = 0; t < NT; ++t) {
        if (t - 3 + OFF >= NREG && t - 3 + OFF < NT) waitvm<16>();
        BAR();
        const int u = t + OFF;
        if (u >= 4 && u < NREG) write_tile(u);
        else if (u >= NREG && u < NT) dma_tile(u);
        if (t == 14 || t == 18 || t == 22 || t == 26) read_w((t - 14) >> 2);
        if (t == 10 || t == 14 || t == 18 || t == 22) issue_w((t - 10) >> 2);
        if (t == 27) issue_wol(3, EX);
        if (t == 29) issue_wol(0, KR);
        if (t == 30) issue_wol(1, VR);
        if (t == 31) issue_wol(2, KR + 2 * TB);
      }
    };
    if (w < 2) stream(std::integral_constant<int, 4>{}); else stream(std::integral_constant<int, 3>{});
    BAR();
    BAR();
    DRAIN(); BAR();
    const u64 tag = *(volatile LAS u64*)(size_t)(L0 + MISC);
    f32x16 acc[2] = {f32x16{}, f32x16{}};
    {
      bf16x8 B[16];
#pragma unroll
      for (int j = 0; j < 16; ++j) B[j] = lds128(ofrag(4u * ((u32)((j >> 2) + b) & 3u) + (j & 3)));
#pragma unroll
      for (int j = 0; j < 16; ++j) { acc[0] = MFMA(wr[0][j], B[j], acc[0]); acc[1] = MFMA(wr[1][j], B[j], acc[1]); }
    }
#pragma unroll
    for (int n = 0; n < 2; ++n)
#pragma unroll
      for (int u = 0; u < 2; ++u)
        __builtin_amdgcn_raw_buffer_store_b128(__builtin_bit_cast(u32x4, pack8(acc[n], u)), sW, 1024u * u + 16u * lane + WS_SL + (u32)((g * 16 + 4 * w + n) * 16 + si) * 2048u, 0, AUX_SC1);
    DRAIN();
    if (lane == 0) __hip_atomic_store((GAS u64*)(ws + WS_F3 + (g * 8 + wave) * 128) + si, tag, RLX_AGENT);
    return;
  }

  const int w = wave;
  St c;
  u64 tag;
  {
    const __amdgpu_buffer_rsrc_t sX = __builtin_amdgcn_make_buffer_rsrc((char*)P.x[pi] + (size_t)b * 32768, 0, 32768, 0x00020000);
    const __amdgpu_buffer_rsrc_t sQ = __builtin_amdgcn_make_buffer_rsrc((char*)P.wq[pi] + (size_t)(kvh * 512 + hh * 256) * 2, 0, 511 * 8192 + 512, 0x00020000);
    const u32 vq = (u32)(lane >> 3) * 8192u + 16u * ((u32)(lane & 7) ^ (u32)(((lane >> 4) & 1) << 2)) + 128u * (u32)w;
    auto issue_wq = [&](int cc) {
      const u32 ch = (u32)(cc + b) & 7u;
#pragma unroll
      for (int i = 0; i < 8; ++i) DMAW(L0 + 32768u * w + 8192u * (cc & 3) + 1024u * i, vq, sQ, (64u * ch + 8u * i) * 8192u);
    };
    {
      const GAS u64* ep = (const GAS u64*)(ws + WS_EP + g * 256);
      asm volatile("global_load_dwordx2 %0, %1, off sc1" : "=v"(tag) : "v"(ep) : "memory");
    }
#pragma unroll
    for (int i = 0; i < 8; ++i) { const u32 q = 8u * w + i; DMAW(L0 + EX + 1024u * q, (16u * lane) ^ (16u * (q & 15u)), sX, 1024u * q); }
    issue_wq(0); issue_wq(1); issue_wq(2); issue_wq(3);
    BAR();
    waitvm<32>(); BAR();
    asm volatile("" : "+v"(tag));
    tag += TAGC;
    bf16x8 xf[32];
#pragma unroll
    for (int j = 0; j < 32; ++j) {
      const u32 ks = 4u * ((u32)((j >> 2) + b) & 7u) + (j & 3);
      xf[j] = lds128(L0 + EX + 1024u * r + ((32u * ks + 16u * h) ^ (16u * (r & 15))));
    }
    BAR();
    f32x16 qa[2] = {f32x16{}, f32x16{}};
    const u32 aq0 = L0 + 32768u * w + 128u * (8u * h + q4) + 16u * ((2u * blk + (p4 & 1)) ^ (u32)((q4 >> 1) << 2)) + 8u * (p4 >> 1);
#pragma unroll
    for (int cc = 0; cc < 8; ++cc) {
      if (cc <= 4) waitvm<24>(); else if (cc == 5) waitvm<16>(); else if (cc == 6) waitvm<8>(); else waitvm<0>();
      bf16x8 af[8];
#pragma unroll
      for (int kk = 0; kk < 4; ++kk)
#pragma unroll
        for (int T = 0; T < 2; ++T) { const u32 a = (aq0 ^ (64u * T)) + 8192u * (cc & 3) + 2048u * kk; af[2 * kk + T] = trfrag(a, a + 512u); }
      LGKM0();
      if (cc < 4) issue_wq(cc + 4);
      if (cc == 3) BAR();
      SBAR();
#pragma unroll
      for (int kk = 0; kk < 4; ++kk)
#pragma unroll
        for (int T = 0; T < 2; ++T) qa[T] = MFMA(af[2 * kk + T], xf[4 * cc + kk], qa[T]);
    }
    const u32 fo = (u32)((w >> 1) * 8 + 4 * (w & 1)) * 1024u + 16u * lane;
#pragma unroll
    for (int T = 0; T < 2; ++T)
#pragma unroll
      for (int s = 0; s < 2; ++s) lds_st128(L0 + EX + fo + 1024u * (2 * T + s), pack8(qa[T], s));
  }
  const int hl = w & 1, kbw = w >> 1;
  BAR();
#pragma unroll
  for (int ks = 0; ks < 8; ++ks) c.qf[ks] = lds128(L0 + EX + (u32)(hl * 8 + ks) * 1024u + 16u * lane);
#pragma unroll
  for (int e = 0; e < 2; ++e) {
    c.ak[e] = 8192u * kbw + 2048u * (u32)(r >> 3) + 64u * (u32)(r & 7) + 16u * ((u32)(2 * e + h) ^ (u32)((r >> 2) & 3));
    c.av[e] = 8192u * kbw + 2048u * e + 64u * (u32)(4 * h + q4) + 16u * ((u32)(2 * blk + (p4 >> 1)) ^ (u32)((2 * e + h) & 3)) + 8u * (u32)(p4 & 1);
  }
#pragma unroll
  for (int d = 0; d < 4; ++d) c.o[d] = f32x16{};
  c.l = 0.f; c.bs[0] = c.bs[1] = 0.f; c.pend = false; c.al = 1.f; c.mx = 0.f;
  BAR();
  {
    c.sA = f32x16{};
#pragma unroll
    for (int i = 0; i < 8; ++i) c.sA = MFMA(lds128(L0 + KR + c.ak[i & 1] + 512u * (i >> 1)), c.qf[i], c.sA);
    float mx = c.sA[0];
#pragma unroll
    for (int i = 1; i < 16; ++i) mx = fmaxf(mx, c.sA[i]);
    c.mc = swap_max(mx) * CSC;
  }
#pragma unroll 1
  for (int t = 0; t < NT - 2; t += 2) {
    tile_step<true>(c, c.sA, c.sB, L0, t);
    tile_step<true>(c, c.sB, c.sA, L0, t + 1);
  }
  tile_step<true>(c, c.sA, c.sB, L0, NT - 2);
  tile_step<false>(c, c.sB, c.sA, L0, NT - 1);
  const float l = swap_add(c.l), mc = c.mc;
  auto merge = [&](auto kbc) {
    constexpr int KB = decltype(kbc)::value;
    const float inv = 1.0f / l;
    const u32 reg = L0 + MS + 8192u * w, rin = L0 + MS + 8192u * (w ^ 2);
#pragma unroll
    for (int j = 0; j < 4; ++j) {
      const f32x16& t = c.o[2 * (1 - KB) + (j >> 1)];
      u32x4 f;
#pragma unroll
      for (int e = 0; e < 4; ++e) f[e] = cvtpk(t[8 * (j & 1) + 2 * e] * inv, t[8 * (j & 1) + 2 * e + 1] * inv);
      lds_st128u(reg + 1024u * j + 16u * lane, f);
    }
    if (lane < 32) *(LAS f32x2*)(size_t)(reg + 4096u + 8u * lane) = f32x2{mc, l};
    if (w == 0 && lane == 0) *(volatile LAS u64*)(size_t)(L0 + MISC) = tag;
    BAR();
    const f32x2 st = *(const LAS f32x2*)(size_t)(rin + 4096u + 8u * r);
    u32x4 f[4];
#pragma unroll
    for (int j = 0; j < 4; ++j) f[j] = lds128u(rin + 1024u * j + 16u * lane);
    const float mn = fmaxf(mc, st[0]);
    const float a1 = __builtin_amdgcn_exp2f(mc - mn), a2 = __builtin_amdgcn_exp2f(st[0] - mn) * st[1];
    const float iv = 1.0f / (l * a1 + a2);
    const float f1 = a1 * iv, f2 = a2 * iv;
#pragma unroll
    for (int j = 0; j < 4; ++j) {
      const f32x16& t = c.o[2 * KB + (j >> 1)];
      u32x4 o4;
#pragma unroll
      for (int e = 0; e < 4; ++e)
        o4[e] = cvtpk(t[8 * (j & 1) + 2 * e] * f1 + __uint_as_float(f[j][e] << 16) * f2, t[8 * (j & 1) + 2 * e + 1] * f1 + __uint_as_float(f[j][e] & 0xffff0000u) * f2);
      lds_st128u(rin + 1024u * j + 16u * lane, o4);
    }
  };
  if (kbw) merge(std::integral_constant<int, 1>{}); else merge(std::integral_constant<int, 0>{});
  BAR();
  {
    f32x16 acc[2] = {f32x16{}, f32x16{}};
    const u32 ao0 = L0 + 512u * (4u * h + q4) + 16u * ((8u * w + 2u * blk + (p4 >> 1)) ^ (4u * q4)) + 8u * (p4 & 1);
#pragma unroll
    for (int cc = 0; cc < 4; ++cc) {
      bf16x8 B[4], A[8];
      const u32 buf = cc == 0 ? KR : cc == 1 ? VR : cc == 2 ? KR + 2 * TB : EX;
#pragma unroll
      for (int kk = 0; kk < 4; ++kk) {
        B[kk] = lds128(ofrag(4u * ((u32)(cc + b) & 3u) + kk));
#pragma unroll
        for (int T = 0; T < 2; ++T) { const u32 a = buf + (ao0 ^ (64u * T)) + 8192u * kk; A[2 * kk + T] = trfrag(a, a + 4096u); }
      }
#pragma unroll
      for (int kk = 0; kk < 4; ++kk) { acc[0] = MFMA(A[2 * kk], B[kk], acc[0]); acc[1] = MFMA(A[2 * kk + 1], B[kk], acc[1]); }
    }
#pragma unroll
    for (int n = 0; n < 2; ++n)
#pragma unroll
      for (int u = 0; u < 2; ++u)
        __builtin_amdgcn_raw_buffer_store_b128(__builtin_bit_cast(u32x4, pack8(acc[n], u)), sW, 1024u * u + 16u * lane + WS_SL + (u32)((g * 16 + 4 * w + 2 + n) * 16 + si) * 2048u, 0, AUX_SC1);
    DRAIN();
    if (lane == 0) __hip_atomic_store((GAS u64*)(ws + WS_F3 + (g * 8 + wave) * 128) + si, tag, RLX_AGENT);
  }
  poll_flags((const GAS u64*)(ws + WS_F3 + (g * 8 + (si >> 2) + ((si & 2) ? 0 : 4)) * 128), 15, lane, tag);
  f32x4 sum = {0.f, 0.f, 0.f, 0.f};
  {
    u32x2 v[16];
#pragma unroll
    for (int i = 0; i < 16; ++i)
      v[i] = __builtin_amdgcn_raw_buffer_load_b64(sW, 1024u * (w >> 1) + 16u * lane + 8u * (w & 1) + 2048u * i, WS_SL + (u32)((g * 16 + si) * 16) * 2048u, AUX_SC1);
#pragma unroll
    for (int i = 0; i < 16; ++i) {
      sum[0] += __uint_as_float(v[i][0] << 16); sum[1] += __uint_as_float(v[i][0] & 0xffff0000u);
      sum[2] += __uint_as_float(v[i][1] << 16); sum[3] += __uint_as_float(v[i][1] & 0xffff0000u);
    }
  }
  u32x2 ov = {cvtpk(sum[0], sum[1]), cvtpk(sum[2], sum[3])};
  *(GAS u32x2*)((char*)P.out + ((size_t)(b * 32 + r) * 1024 + pi * 512 + 32 * si + 8 * w + 4 * h) * 2) = ov;
  if (w == 0 && lane == 0) __hip_atomic_store((GAS u64*)(ws + WS_EP + g * 256), tag, RLX_AGENT);
}

extern "C" void kernel_launch(void* const* d_in, const int* in_sizes, int n_in, void* d_out, int out_size, void* d_ws, size_t ws_size, hipStream_t stream) {
  (void)hipFuncSetAttribute((const void*)gqa_block_fused, hipFuncAttributeMaxDynamicSharedMemorySize, (int)LDS_BYTES);
  Params P;
  for (int p = 0; p < 2; ++p) { P.x[p] = d_in[5 * p + 0]; P.k[p] = d_in[5 * p + 1]; P.v[p] = d_in[5 * p + 2]; P.wq[p] = d_in[5 * p + 3]; P.wo[p] = d_in[5 * p + 4]; }
  P.out = d_out; P.ws = d_ws;
  hipLaunchKernelGGL(gqa_block_fused, dim3(256), dim3(512), LDS_BYTES, stream, P);
}
```
